```python
import torch

def _softmax(x, axis=-1):
    return torch.softmax(x, dim=axis)
def _gelu(x):
    return torch.nn.functional.gelu(x, approximate="tanh")
def _silu(x):
    return torch.nn.functional.silu(x)
def _sigmoid(x):
    return torch.sigmoid(x)
def _layernorm(x, axis=-1, eps=1e-5):
    m = x.mean(dim=axis, keepdim=True)
    v = x.var(dim=axis, keepdim=True, unbiased=False)
    return (x - m) / torch.sqrt(v + eps)
def _scatter_add(tgt, idx, src, axis=0):
    return tgt.clone().index_add_(axis, idx, src)
def _segment_sum(src, seg, n):
    out = torch.zeros((n,) + src.shape[1:], dtype=src.dtype)
    return out.index_add_(0, seg, src)
def _pad(x, pw):
    flat = tuple(v for pair in reversed(pw) for v in pair)
    return torch.nn.functional.pad(x, flat)

kernel_name = 'sequential_preLN__sliding_block__geglu_ffn__prompt'

def setup_inputs():

    _w = (lambda a: (lambda t: t.to(torch.bfloat16) if t.is_floating_point() else t)(torch.as_tensor(a)))
    def _A_setup():
        B, S, D = 1, 2048, 640
        return {
            "x": _w((lambda *s: torch.randn(*s, dtype=torch.bfloat16) * 0.05)(B, S, D)),
            "wqkv": _w((lambda *s: torch.randn(*s, dtype=torch.bfloat16) * 0.05)(D, 3 * D)),
            "wo": _w((lambda *s: torch.randn(*s, dtype=torch.bfloat16) * 0.05)(D, D)),
        }
    _A_in = _A_setup()

    def _B_setup():
        B, S, D, F = 1, 2048, 640, 1728
        return {
            "x": _w((lambda *s: torch.randn(*s, dtype=torch.bfloat16) * 0.05)(B, S, D)),
            "w_gate": _w((lambda *s: torch.randn(*s, dtype=torch.bfloat16) * 0.05)(D, F)),
            "w_up": _w((lambda *s: torch.randn(*s, dtype=torch.bfloat16) * 0.05)(D, F)),
            "w_down": _w((lambda *s: torch.randn(*s, dtype=torch.bfloat16) * 0.05)(F, D)),
        }
    _B_in = _B_setup()

    return {
        "A_x": _A_in["x"],
        "A_wqkv": _A_in["wqkv"],
        "A_wo": _A_in["wo"],
        "B_x": _B_in["x"],
        "B_w_gate": _B_in["w_gate"],
        "B_w_up": _B_in["w_up"],
        "B_w_down": _B_in["w_down"],
    }

def reference(A_x, A_wqkv, A_wo, B_x, B_w_gate, B_w_up, B_w_down):
    def _A_ref(x, wqkv, wo):
        B, S, D = x.shape
        H = 32; Dh = D // H; W = 256
        qkv = (lambda x, *d: x.permute(*d))((x @ wqkv).reshape(B, S, 3, H, Dh), 2, 0, 3, 1, 4)
        q, k, v = qkv[0], qkv[1], qkv[2]
        scores = torch.einsum("bhqd,bhkd->bhqk", q, k) * (Dh ** -0.5)
        idx = torch.arange(S)
        mask = (idx[:, None] - idx[None, :] >= 0) & (idx[:, None] - idx[None, :] < W)
        scores = torch.where(mask[None, None], scores, -1e9)
        p = _softmax(scores, axis=-1)
        o = torch.einsum("bhqk,bhkd->bhqd", p, v)
        return (lambda x, *d: x.permute(*d))(o, 0, 2, 1, 3).reshape(B, S, D) @ wo

    def _B_ref(x, w_gate, w_up, w_down):
        return (_gelu(x @ w_gate) * (x @ w_up)) @ w_down

    x = A_x
    h = x + _A_ref(_layernorm(x, axis=-1), A_wqkv, A_wo)
    h2 = h + _B_ref(_layernorm(h, axis=-1), B_w_gate, B_w_up, B_w_down)
    return h2
```

```cpp
#include <hip/hip_runtime.h>
#include <hip/hip_bf16.h>
#include <stdint.h>

#define LAS __attribute__((address_space(3)))
typedef short bf16x8 __attribute__((ext_vector_type(8)));
typedef short s16x4 __attribute__((ext_vector_type(4)));
typedef float f32x2 __attribute__((ext_vector_type(2)));
typedef float f32x4 __attribute__((ext_vector_type(4)));
typedef float f32x16 __attribute__((ext_vector_type(16)));
typedef unsigned u32x2 __attribute__((ext_vector_type(2)));
typedef unsigned u32x4 __attribute__((ext_vector_type(4)));
typedef LAS char* lptr;

constexpr int S_ = 2048, D_ = 640, H_ = 32, F_ = 1728;
constexpr float LN_EPS = 1e-5f;
constexpr float QSCALE = 0.22360679774997896f * 1.4426950408889634f;

constexpr size_t WS_QKV = 0;
constexpr size_t WS_O   = WS_QKV + (size_t)3 * 32 * 128 * 640;
constexpr size_t WS_H   = WS_O + (size_t)128 * 20 * 1024;
constexpr size_t WS_G   = WS_H + (size_t)128 * 20 * 1024;
constexpr size_t WS_WO  = WS_G + (size_t)128 * 54 * 1024;
constexpr size_t WS_WD  = WS_WO + (size_t)8 * 20 * 5 * 1024;
constexpr size_t WS_WGU = WS_WD + (size_t)8 * 54 * 5 * 1024;
constexpr size_t WS_HST = WS_WGU + (size_t)20 * 108 * 2 * 1024 + 4096;
constexpr size_t WS_CS  = WS_HST + (size_t)128 * 8 * 16 * 8;
constexpr size_t WS_END = WS_CS + (size_t)4 * 2 * 1728 * 4 + 4096;

__device__ __forceinline__ unsigned cvtpk(float lo, float hi) { unsigned r; asm("v_cvt_pk_bf16_f32 %0, %1, %2" : "=v"(r) : "v"(lo), "v"(hi)); return r; }
__device__ __forceinline__ float bflo(unsigned v) { return __uint_as_float(v << 16); }
__device__ __forceinline__ float bfhi(unsigned v) { return __uint_as_float(v & 0xffff0000u); }
__device__ __forceinline__ s16x4 trread(lptr p) { return __builtin_amdgcn_ds_read_tr16_b64_v4i16((LAS s16x4*)p); }
__device__ __forceinline__ bf16x8 cat8(s16x4 a, s16x4 b) { return __builtin_shufflevector(a, b, 0, 1, 2, 3, 4, 5, 6, 7); }
#define MFMA16(a, b, c) __builtin_amdgcn_mfma_f32_16x16x32_bf16(a, b, c, 0, 0, 0)
#define MFMA32(a, b, c) __builtin_amdgcn_mfma_f32_32x32x16_bf16(a, b, c, 0, 0, 0)
#define MFMA32K8(a, b, c) __builtin_amdgcn_mfma_f32_32x32x8bf16_1k(a, b, c, 0, 0, 0)
__device__ __forceinline__ void glds(unsigned voff, const void* sbase, unsigned ldsaddr) {
  asm volatile("s_mov_b32 m0, %2\n\ts_nop 0\n\tglobal_load_lds_dwordx4 %0, %1" :: "v"(voff), "s"(sbase), "s"(ldsaddr) : "memory");
}
#define WAITV_BAR(N) asm volatile("s_waitcnt vmcnt(" #N ") lgkmcnt(0)\n\ts_barrier" ::: "memory")
template <int N> __device__ __forceinline__ void waitv_bar() { asm volatile("s_waitcnt vmcnt(%0) lgkmcnt(0)\n\ts_barrier" :: "i"(N) : "memory"); }
__device__ __forceinline__ unsigned rfl(unsigned v) { return __builtin_amdgcn_readfirstlane(v); }
__device__ __forceinline__ float shx(float v, int m) { return __shfl_xor(v, m); }

constexpr int K1_STAGE = 32768, K1_RING = 4, K1_LDS = K1_STAGE * K1_RING + 2048;
__global__ __launch_bounds__(256) void k1_qkv(const unsigned short* __restrict__ x, const unsigned short* __restrict__ wqkv, char* __restrict__ ws) {
  extern __shared__ __attribute__((aligned(16))) char smem[];
  const int bid = blockIdx.x, xcd = bid & 7, idx = bid >> 3;
  const int tm = 4 * (xcd >> 1) + (idx & 3), tn = 8 * (xcd & 1) + (idx >> 2);
  if (tn >= 15) return;
  const int tid = threadIdx.x, lane = tid & 63, wid = rfl(tid >> 6), wr = wid >> 1, wc = wid & 1;
  const int r = lane & 15, g = lane >> 4;
  const lptr L = (lptr)smem;
  const unsigned lds0 = (unsigned)(uintptr_t)L;
  unsigned voffX[4], voffW[4];
#pragma unroll
  for (int i = 0; i < 4; ++i) {
    const int p = wid * 4 + i;
    voffX[i] = (unsigned)((8 * p + (lane >> 3)) * 1280 + (((lane & 7) ^ (lane >> 3)) * 16));
    const int f = (lane >> 4) | (((p >> 1) & 1) << 2), p16 = lane & 15;
    voffW[i] = (unsigned)((4 * p + (lane >> 4)) * 3840 + (((((p16 >> 1) ^ f) << 1) | (p16 & 1)) * 16));
  }
  const char* xb = (const char*)x + (size_t)tm * 128 * 1280;
  const char* wb = (const char*)wqkv + tn * 256;
  auto stage = [&](int st, int slot) {
    const char* xs = xb + st * 128;
    const char* wsrc = wb + (size_t)st * 64 * 3840;
    const unsigned d = lds0 + slot * K1_STAGE + wid * 4096;
#pragma unroll
    for (int i = 0; i < 4; ++i) glds(voffX[i], xs, d + i * 1024);
#pragma unroll
    for (int i = 0; i < 4; ++i) glds(voffW[i], wsrc, d + 16384 + i * 1024);
  };
  stage(0, 0); stage(1, 1); stage(2, 2);
  const int xo0 = (64 * wr + r) * 128 + ((g ^ (r & 7)) * 16);
  const int xoA = xo0 + 4096 * wc, xoB = xo0 + 4096 * (1 - wc);
  int wo[4];
  { const int q = (lane & 15) >> 2, p = lane & 3, f = q | ((g & 1) << 2);
#pragma unroll
    for (int n = 0; n < 4; ++n) wo[n] = 16384 + (8 * g + q) * 256 + (((4 * wc + (n ^ (2 * wr))) ^ f) * 32) + p * 8; }
  f32x4 acc[4][4], ssum[2], gram[2], csum[2];
#pragma unroll
  for (int n = 0; n < 4; ++n)
#pragma unroll
    for (int m = 0; m < 4; ++m) acc[n][m] = f32x4{0.f, 0.f, 0.f, 0.f};
#pragma unroll
  for (int i = 0; i < 2; ++i) { ssum[i] = f32x4{0.f, 0.f, 0.f, 0.f}; gram[i] = ssum[i]; csum[i] = ssum[i]; }
  const bf16x8 ones = {0x3F80, 0x3F80, 0x3F80, 0x3F80, 0x3F80, 0x3F80, 0x3F80, 0x3F80};
  for (int st = 0; st < 10; ++st) {
    if (st < 8) WAITV_BAR(16); else if (st == 8) WAITV_BAR(8); else WAITV_BAR(0);
    if (st + 3 < 10) stage(st + 3, (st + 3) & 3);
    const lptr B = L + (st & 3) * K1_STAGE;
#pragma unroll
    for (int kk = 0; kk < 2; ++kk) {
      bf16x8 xf[4], wf[4];
      xf[0] = *(LAS bf16x8*)(B + (xoA ^ (64 * kk)));
      xf[1] = *(LAS bf16x8*)(B + (xoA ^ (64 * kk)) + 2048);
      xf[2] = *(LAS bf16x8*)(B + (xoB ^ (64 * kk)));
      xf[3] = *(LAS bf16x8*)(B + (xoB ^ (64 * kk)) + 2048);
#pragma unroll
      for (int n = 0; n < 4; ++n) wf[n] = cat8(trread(B + wo[n] + 8192 * kk), trread(B + wo[n] + 8192 * kk + 1024));
#pragma unroll
      for (int n = 0; n < 4; ++n)
#pragma unroll
        for (int m = 0; m < 4; ++m) acc[n][m] = MFMA16(wf[n], xf[m], acc[n][m]);
#pragma unroll
      for (int i = 0; i < 2; ++i) {
        ssum[i] = MFMA16(ones, xf[i], ssum[i]);
        gram[i] = MFMA16(xf[i], xf[i], gram[i]);
        csum[i] = MFMA16(wf[i], ones, csum[i]);
      }
    }
  }
  LAS f32x2* st_tok = (LAS f32x2*)(L + K1_STAGE * K1_RING);
  LAS float* st_col = (LAS float*)(L + K1_STAGE * K1_RING + 1024);
#pragma unroll
  for (int i = 0; i < 2; ++i) {
    const int j = r & 3;
    const float sq = j == 0 ? gram[i][0] : j == 1 ? gram[i][1] : j == 2 ? gram[i][2] : gram[i][3];
    const float mean = ssum[i][0] * (1.0f / D_);
    const float var = fmaxf(sq * (1.0f / D_) - mean * mean, 0.f);
    if (g == (r >> 2)) st_tok[64 * wr + 16 * (2 * wc + i) + r] = f32x2{mean, __builtin_amdgcn_rsqf(var + LN_EPS)};
    if (r == 0) *(LAS f32x4*)(st_col + 64 * wc + 16 * (2 * wr + i) + 4 * g) = csum[i];
  }
  __syncthreads();
  const int tensor = tn / 5;
  const float osc = tensor == 0 ? QSCALE : 1.0f;
  f32x2 ts[4];
#pragma unroll
  for (int m = 0; m < 4; ++m) { ts[m] = st_tok[64 * wr + 16 * (m ^ (2 * wc)) + r]; ts[m].y *= osc; }
  char* qkv = ws + WS_QKV;
#pragma unroll
  for (int n = 0; n < 4; ++n) {
    const int np = n ^ (2 * wr);
    const f32x4 cs = *(LAS f32x4*)(st_col + 64 * wc + 16 * np + 4 * g);
    const int c4 = (tn - 5 * tensor) * 32 + 16 * wc + 4 * np + g;
    const int head = c4 / 5, dg = c4 - 5 * head;
    char* dst = qkv + ((size_t)(tensor * 32 + head) * 128 + 8 * tm + 4 * wr) * 640 + dg * 128 + r * 8;
#pragma unroll
    for (int m = 0; m < 4; ++m) {
      const f32x4 v = (acc[n][m] - ts[m].x * cs) * ts[m].y;
      u32x2 o; o.x = cvtpk(v[0], v[1]); o.y = cvtpk(v[2], v[3]);
      *(u32x2*)(dst + (m ^ (2 * wc)) * 640) = o;
    }
  }
}

constexpr int K2_K = 0, K2_V = 20480, K2_Q = 40960, K2_ONE = 51200, K2_RT = 51200 + 256, K2_CS = K2_RT + 8 * 4096, K2_LDS = K2_CS + 5 * 64 * 4;
__global__ __launch_bounds__(512) void k2_attn(const unsigned short* __restrict__ wo, const unsigned short* __restrict__ wg, const unsigned short* __restrict__ wu,
                                               const unsigned short* __restrict__ wd, char* __restrict__ ws) {
  extern __shared__ __attribute__((aligned(16))) char smem[];
  const int bid = blockIdx.x, xcd = bid & 7, idx = bid >> 3;
  const int head = 4 * xcd + (idx >> 3), qb = idx & 7;
  const int tid = threadIdx.x, lane = tid & 63, wid = rfl(tid >> 6);
  const lptr L = (lptr)smem;
  const unsigned lds0 = (unsigned)(uintptr_t)L;
  const char* qkv = ws + WS_QKV;
  {
    const char* qsrc = qkv + ((size_t)(0 * 32 + head) * 128 + 16 * qb) * 640;
    const char* ksrc = qkv + ((size_t)(1 * 32 + head) * 128 + 16 * qb) * 640;
    const char* vsrc = qkv + ((size_t)(2 * 32 + head) * 128 + 16 * qb) * 640;
    const int prev = qb > 0 ? -10240 : 0;
    const unsigned vo = lane * 16;
#pragma unroll
    for (int i = 0; i < 7; ++i) {
      const int p = wid + 8 * i;
      if (p < 50) {
        const int grp = p / 10, e = p - 10 * grp;
        const char* src = grp == 0 ? ksrc : grp == 1 ? qsrc : grp == 2 ? ksrc + prev : grp == 3 ? vsrc + prev : vsrc;
        const int dst = grp == 0 ? K2_K + 10240 : grp == 1 ? K2_Q : grp == 2 ? K2_K : grp == 3 ? K2_V : K2_V + 10240;
        glds(vo, src + e * 1024, lds0 + dst + e * 1024);
      }
    }
  }
  const char* usrc; char* udst; int uld, ustride; bool uvalid = true;
  if (wid < 5) {
    uvalid = bid < 216;
    const int cb = (bid < 216 ? bid : 0) >> 2, s = 5 * (bid & 3) + wid, isup = cb >= 27, cbb = cb - 27 * isup;
    usrc = (const char*)(isup ? wu : wg) + (size_t)s * 32 * 3456 + cbb * 128; uld = 3456;
    udst = ws + WS_WGU + ((size_t)(s * 108 + 4 * cbb) * 2 + isup) * 1024; ustride = 2048;
  } else {
    int u = bid * 3 + (wid - 5); uvalid = u < 740; if (!uvalid) u = 0;
    const bool isd = u < 540; const int uu = isd ? u : u - 540; const int s = uu / 10, cb = uu - 10 * s;
    usrc = (const char*)(isd ? wd : wo) + (size_t)s * 32 * 1280 + cb * 128; uld = 1280;
    udst = ws + (isd ? WS_WD : WS_WO) + (size_t)s * 5 * 1024; ustride = (isd ? 54 : 20) * 5 * 1024 + cb * (1 << 24);
  }
  {
    const unsigned vo = (lane >> 3) * uld + (lane & 7) * 16;
#pragma unroll
    for (int i = 0; i < 4; ++i) glds(vo, usrc + (size_t)8 * i * uld, lds0 + K2_RT + wid * 4096 + i * 1024);
  }
  if (tid < 32) *(LAS u32x2*)(L + K2_ONE + tid * 8) = u32x2{0x00003F80u, 0u};
  WAITV_BAR(4);

  const int c = lane & 31, hh = lane >> 5;
  const int blk0 = wid;
  const int nskip = qb == 0 ? 8 - wid : 0;
  bf16x8 q1; s16x4 q2;
  { const lptr qp = L + K2_Q + (2 * wid + (c >> 4)) * 640 + (c & 15) * 8;
    q1 = cat8(*(LAS s16x4*)(qp + 256 * hh), *(LAS s16x4*)(qp + 256 * hh + 128));
    q2 = *(LAS s16x4*)(qp + 512); if (hh) q2 = s16x4{0, 0, 0, 0}; }
  const lptr kp = L + K2_K + (2 * blk0 + (c >> 4)) * 640 + (c & 15) * 8;
  f32x16 s[9];
  const f32x16 zero16 = {0.f,0.f,0.f,0.f,0.f,0.f,0.f,0.f,0.f,0.f,0.f,0.f,0.f,0.f,0.f,0.f};
#pragma unroll
  for (int jb = 0; jb < 9; ++jb) {
    const bf16x8 k1 = cat8(*(LAS s16x4*)(kp + jb * 1280 + 256 * hh), *(LAS s16x4*)(kp + jb * 1280 + 256 * hh + 128));
    const s16x4 k2 = *(LAS s16x4*)(kp + jb * 1280 + 512);
    s[jb] = MFMA32(k1, q1, zero16);
    s[jb] = MFMA32K8(k2, q2, s[jb]);
  }
  if (nskip > 0) {
#pragma unroll
    for (int jb = 0; jb < 8; ++jb)
      if (jb < nskip) {
#pragma unroll
        for (int e = 0; e < 16; ++e) s[jb][e] = -INFINITY;
      }
  }
#pragma unroll
  for (int e = 0; e < 16; ++e) { const int kk = (e & 3) + 8 * (e >> 2) + 4 * hh; s[0][e] = kk > c ? s[0][e] : s[8][e]; }
  float m0 = s[0][0], m1 = s[0][1];
#pragma unroll
  for (int jb = 0; jb < 8; ++jb)
#pragma unroll
    for (int e = (jb == 0 ? 2 : 0); e < 16; e += 4) {
      m0 = fmaxf(fmaxf(m0, s[jb][e]), s[jb][e + 1]);
      if (e + 2 < 16) m1 = fmaxf(fmaxf(m1, s[jb][e + 2]), s[jb][e + 3]);
    }
  float mx = fmaxf(m0, m1);
  { auto rr = __builtin_amdgcn_permlane32_swap(__float_as_uint(mx), __float_as_uint(mx), false, false);
    mx = fmaxf(__uint_as_float(rr[0]), __uint_as_float(rr[1])); }
  const int gq = (lane & 15) >> 2, gp = lane & 3, up16 = (lane >> 4) & 1;
  const bool vconst = up16 && gp > 0;
  const int vbase = vconst ? K2_ONE + gq * 8 : K2_V + 2 * blk0 * 640 + (4 * up16 + gp) * 128 + (4 * hh + gq) * 8;
  const int vstep = vconst ? 0 : 640;
  f32x16 o = zero16;
  auto pv = [&](const u32x4 pa, const u32x4 pb, int jb) {
    const lptr v0 = L + vbase + (2 * jb) * vstep, v1 = L + vbase + (2 * jb + 1) * vstep;
    const bf16x8 va = cat8(trread(v0), trread(v0 + 64)), vb = cat8(trread(v1), trread(v1 + 64));
    o = MFMA32(va, __builtin_bit_cast(bf16x8, pa), o);
    o = MFMA32(vb, __builtin_bit_cast(bf16x8, pb), o);
  };
#pragma unroll
  for (int jb = 0; jb < 8; ++jb) {
    float p[16];
#pragma unroll
    for (int e = 0; e < 16; ++e) p[e] = __builtin_amdgcn_exp2f(s[jb][e] - mx);
    if (jb == 0) {
      float pf[16], pl[16];
#pragma unroll
      for (int e = 0; e < 16; ++e) { const int kk = (e & 3) + 8 * (e >> 2) + 4 * hh; pf[e] = kk > c ? p[e] : 0.f; pl[e] = kk > c ? 0.f : p[e]; }
      pv(u32x4{cvtpk(pf[0], pf[1]), cvtpk(pf[2], pf[3]), cvtpk(pf[4], pf[5]), cvtpk(pf[6], pf[7])},
         u32x4{cvtpk(pf[8], pf[9]), cvtpk(pf[10], pf[11]), cvtpk(pf[12], pf[13]), cvtpk(pf[14], pf[15])}, 0);
      pv(u32x4{cvtpk(pl[0], pl[1]), cvtpk(pl[2], pl[3]), cvtpk(pl[4], pl[5]), cvtpk(pl[6], pl[7])},
         u32x4{cvtpk(pl[8], pl[9]), cvtpk(pl[10], pl[11]), cvtpk(pl[12], pl[13]), cvtpk(pl[14], pl[15])}, 8);
    } else {
      pv(u32x4{cvtpk(p[0], p[1]), cvtpk(p[2], p[3]), cvtpk(p[4], p[5]), cvtpk(p[6], p[7])},
         u32x4{cvtpk(p[8], p[9]), cvtpk(p[10], p[11]), cvtpk(p[12], p[13]), cvtpk(p[14], p[15])}, jb);
    }
  }
  {
    const float rl = __builtin_amdgcn_rcpf(hh ? o[8] : o[12]);
    const int T = 16 * qb + 2 * wid + (c >> 4);
    char* ob = ws + WS_O + (size_t)T * 20480 + (c & 15) * 16;
#pragma unroll
    for (int i = 0; i < 3; ++i) {
      const int c4 = 5 * head + 2 * i + hh;
      u32x2 w; w.x = cvtpk(o[4 * i] * rl, o[4 * i + 1] * rl); w.y = cvtpk(o[4 * i + 2] * rl, o[4 * i + 3] * rl);
      if (i < 2 || hh == 0) *(u32x2*)(ob + (c4 >> 3) * 1024 + (c4 & 3) * 256 + ((c4 >> 2) & 1) * 8) = w;
    }
  }
  asm volatile("s_waitcnt vmcnt(0)" ::: "memory");
  {
    const int g = lane >> 4;
    const lptr rp = L + K2_RT + wid * 4096 + (4 * g + gq) * 128 + gp * 8;
    float csv[4];
#pragma unroll
    for (int n = 0; n < 4; ++n) {
      const bf16x8 f = cat8(trread(rp + n * 32), trread(rp + n * 32 + 2048));
      const u32x4 fu = __builtin_bit_cast(u32x4, f);
      char* d;
      if (wid < 5) d = udst + n * ustride;
      else { const int tile = 4 * (ustride >> 24) + n, sl = tile / 5; d = udst + (size_t)sl * (ustride & 0xffffff) + (tile - 5 * sl) * 1024; }
      if (uvalid) *(u32x4*)(d + lane * 16) = fu;
      float cs = ((bflo(fu.x) + bfhi(fu.x)) + (bflo(fu.y) + bfhi(fu.y))) + ((bflo(fu.z) + bfhi(fu.z)) + (bflo(fu.w) + bfhi(fu.w)));
      cs += shx(cs, 16); cs += shx(cs, 32);
      csv[n] = cs;
    }
    if (wid < 5 && lane < 16) {
#pragma unroll
      for (int n = 0; n < 4; ++n) *(LAS float*)(L + K2_CS + (wid * 64 + n * 16 + lane) * 4) = csv[n];
    }
    __syncthreads();
    if (tid < 64 && bid < 216) {
      float t = 0.f;
#pragma unroll
      for (int w = 0; w < 5; ++w) t += *(LAS float*)(L + K2_CS + (w * 64 + tid) * 4);
      const int cb = bid >> 2;
      ((float*)(ws + WS_CS))[(bid & 3) * 3456 + cb * 64 + tid] = t;
    }
  }
}

constexpr int KS_STAGE = 36864, KS_RING = 4, KS_LDS = KS_STAGE * KS_RING;
template <int MODE>
__global__ __launch_bounds__(256) void k_skinny(const unsigned short* __restrict__ x, unsigned short* __restrict__ out, char* __restrict__ ws) {
  constexpr int NS = MODE == 0 ? 20 : 54;
  constexpr int NST = (NS + 3) / 4;
  constexpr int NFULL = NS / 4;
  extern __shared__ __attribute__((aligned(16))) char smem[];
  const int bid = blockIdx.x, xcd = bid & 7, idx = bid >> 3;
  const int rt = 8 * (xcd >> 1) + (idx & 7), ct = 4 * (xcd & 1) + (idx >> 3);
  const int tid = threadIdx.x, lane = tid & 63, wid = rfl(tid >> 6);
  const int r = lane & 15, g = lane >> 4;
  const lptr L = (lptr)smem;
  const unsigned lds0 = (unsigned)(uintptr_t)L;
  const int T = 4 * rt + wid;
  const char* asrc = ws + (MODE == 0 ? WS_O : WS_G) + (size_t)T * NS * 1024;
  const char* wsrc = ws + (MODE == 0 ? WS_WO : WS_WD) + (size_t)ct * NS * 5 * 1024;
  const unsigned vo = lane * 16;
  auto stage = [&](int st) {
    const unsigned d = lds0 + (st & 3) * KS_STAGE;
    if (st < NFULL) {
#pragma unroll
      for (int i = 0; i < 4; ++i) glds(vo, asrc + (st * 4 + i) * 1024, d + 20480 + wid * 4096 + i * 1024);
#pragma unroll
      for (int i = 0; i < 5; ++i) glds(vo, wsrc + (st * 20 + wid * 5 + i) * 1024, d + wid * 5120 + i * 1024);
    } else {
#pragma unroll
      for (int i = 0; i < 2; ++i) glds(vo, asrc + (st * 4 + i) * 1024, d + 20480 + wid * 4096 + i * 1024);
#pragma unroll
      for (int i = 0; i < 3; ++i) if (wid + 4 * i < 10) glds(vo, wsrc + (st * 20 + wid + 4 * i) * 1024, d + (wid + 4 * i) * 1024);
    }
  };
  stage(0); stage(1); stage(2);
  f32x4 acc[5];
#pragma unroll
  for (int n = 0; n < 5; ++n) acc[n] = f32x4{0.f, 0.f, 0.f, 0.f};
  for (int st = 0; st < NFULL; ++st) {
    if (st + 2 < NFULL) WAITV_BAR(18);
    else if (st + 2 == NFULL) { if (NST > NFULL) WAITV_BAR(13); else WAITV_BAR(9); }
    else { if (NST > NFULL) WAITV_BAR(4); else WAITV_BAR(0); }
    if (st + 3 < NST) stage(st + 3);
    const lptr B = L + (st & 3) * KS_STAGE + lane * 16;
#pragma unroll
    for (int kk = 0; kk < 4; ++kk) {
      const bf16x8 af = *(LAS bf16x8*)(B + 20480 + wid * 4096 + kk * 1024);
#pragma unroll
      for (int n = 0; n < 5; ++n) acc[n] = MFMA16(*(LAS bf16x8*)(B + (kk * 5 + n) * 1024), af, acc[n]);
    }
  }
  if (NST > NFULL) {
    WAITV_BAR(0);
    const lptr B = L + (NFULL & 3) * KS_STAGE + lane * 16;
#pragma unroll
    for (int kk = 0; kk < 2; ++kk) {
      const bf16x8 af = *(LAS bf16x8*)(B + 20480 + wid * 4096 + kk * 1024);
#pragma unroll
      for (int n = 0; n < 5; ++n) acc[n] = MFMA16(*(LAS bf16x8*)(B + (kk * 5 + n) * 1024), af, acc[n]);
    }
  }
  const int tok = 16 * T + r;
  char* hb = ws + WS_H + (size_t)T * 20480 + g * 256 + r * 16;
  if (MODE == 0) {
    float s1 = 0.f, s2 = 0.f;
#pragma unroll
    for (int n = 0; n < 5; ++n) {
      const int f = 5 * ct + n;
      const u32x2 xv = *(const u32x2*)(x + (size_t)tok * D_ + 16 * f + 4 * g);
      u32x2 o; o.x = cvtpk(acc[n][0] + bflo(xv.x), acc[n][1] + bfhi(xv.x)); o.y = cvtpk(acc[n][2] + bflo(xv.y), acc[n][3] + bfhi(xv.y));
      *(u32x2*)(hb + (f >> 1) * 1024 + (f & 1) * 8) = o;
      const float a = bflo(o.x), b = bfhi(o.x), c = bflo(o.y), d = bfhi(o.y);
      s1 += (a + b) + (c + d); s2 += (a * a + b * b) + (c * c + d * d);
    }
    s1 += shx(s1, 16); s2 += shx(s2, 16); s1 += shx(s1, 32); s2 += shx(s2, 32);
    if (g == 0) *(f32x2*)(ws + WS_HST + ((size_t)(T * 8 + ct) * 16 + r) * 8) = f32x2{s1, s2};
  } else {
#pragma unroll
    for (int n = 0; n < 5; ++n) {
      const int f = 5 * ct + n;
      const u32x2 hv = *(const u32x2*)(hb + (f >> 1) * 1024 + (f & 1) * 8);
      u32x2 o; o.x = cvtpk(acc[n][0] + bflo(hv.x), acc[n][1] + bfhi(hv.x)); o.y = cvtpk(acc[n][2] + bflo(hv.y), acc[n][3] + bfhi(hv.y));
      *(u32x2*)(out + (size_t)tok * D_ + 16 * f + 4 * g) = o;
    }
  }
}

constexpr int K4_STAGE = 45056, K4_RING = 3, K4_ST = K4_STAGE * K4_RING, K4_CS = K4_ST + 8192, K4_LDS = K4_CS + 8 * 1024;
__global__ __launch_bounds__(256) void k4_gateup(char* __restrict__ ws) {
  extern __shared__ __attribute__((aligned(16))) char smem[];
  const int bid = blockIdx.x, xcd = bid & 7, idx = bid >> 3;
  const int pn = 4 * (xcd >> 1) + (idx & 3), cgp = 8 * (xcd & 1) + (idx >> 2);
  const int t0 = cgp < 12 ? 7 * cgp : 84 + 6 * (cgp - 12), ntile = cgp < 12 ? 7 : 6;
  const int tid = threadIdx.x, lane = tid & 63, wid = rfl(tid >> 6);
  const int r = lane & 15, g = lane >> 4;
  const lptr L = (lptr)smem;
  const unsigned lds0 = (unsigned)(uintptr_t)L;
  const unsigned vo = lane * 16;
  const int T0 = 8 * pn + 2 * wid;
  const char* asrc = ws + WS_H + (size_t)T0 * 20480;
  const char* wsrc = ws + WS_WGU + (size_t)t0 * 2048;
  glds(vo, ws + WS_HST + (size_t)T0 * 1024, lds0 + K4_ST + wid * 2048);
  glds(vo, ws + WS_HST + (size_t)(T0 + 1) * 1024, lds0 + K4_ST + wid * 2048 + 1024);
  glds(vo, ws + WS_CS + (size_t)(wid * 3456 + t0 * 16) * 4, lds0 + K4_CS + wid * 2048);
  glds(vo, ws + WS_CS + (size_t)(wid * 3456 + 1728 + t0 * 16) * 4, lds0 + K4_CS + wid * 2048 + 1024);
  auto stage = [&](int st, int slot) {
    const unsigned d = lds0 + slot * K4_STAGE;
#pragma unroll
    for (int i = 0; i < 4; ++i) glds(vo, asrc + (i >> 1) * 20480 + (st * 2 + (i & 1)) * 1024, d + 28672 + wid * 4096 + i * 1024);
    const char* wk = wsrc + (size_t)(st * 2 + (wid >> 1)) * (108 * 2048) + (wid & 1) * 7168;
#pragma unroll
    for (int i = 0; i < 7; ++i) glds(vo, wk + i * 1024, d + wid * 7168 + i * 1024);
  };
  stage(0, 0); stage(1, 1);
  WAITV_BAR(22);
  float mean[2], rstd[2];
#pragma unroll
  for (int m = 0; m < 2; ++m) {
    const f32x2 a = *(LAS f32x2*)(L + K4_ST + wid * 2048 + m * 1024 + (g * 16 + r) * 8), b = *(LAS f32x2*)(L + K4_ST + wid * 2048 + m * 1024 + ((g + 4) * 16 + r) * 8);
    float s1 = a.x + b.x, s2 = a.y + b.y;
    s1 += shx(s1, 16); s2 += shx(s2, 16); s1 += shx(s1, 32); s2 += shx(s2, 32);
    mean[m] = s1 * (1.0f / D_);
    rstd[m] = __builtin_amdgcn_rsqf(fmaxf(s2 * (1.0f / D_) - mean[m] * mean[m], 0.f) + LN_EPS);
  }
  f32x4 acc[7][2][2];
#pragma unroll
  for (int n = 0; n < 7; ++n)
#pragma unroll
    for (int u = 0; u < 2; ++u) {
      const lptr cp = L + K4_CS + u * 1024 + (n * 16 + 4 * g) * 4;
      const f32x4 cs = (*(LAS f32x4*)(cp) + *(LAS f32x4*)(cp + 2048)) + (*(LAS f32x4*)(cp + 4096) + *(LAS f32x4*)(cp + 6144));
      acc[n][u][0] = cs * (-mean[0]); acc[n][u][1] = cs * (-mean[1]);
    }
  int slot = 0;
  for (int st = 0; st < 10; ++st) {
    if (st < 9) WAITV_BAR(11); else WAITV_BAR(0);
    if (st + 2 < 10) stage(st + 2, slot == 0 ? 2 : slot - 1);
    const lptr B = L + slot * K4_STAGE + lane * 16;
#pragma unroll
    for (int kk = 0; kk < 2; ++kk) {
      const bf16x8 a0 = *(LAS bf16x8*)(B + 28672 + wid * 4096 + kk * 1024), a1 = *(LAS bf16x8*)(B + 28672 + wid * 4096 + 2048 + kk * 1024);
#pragma unroll
      for (int n = 0; n < 7; ++n)
#pragma unroll
        for (int u = 0; u < 2; ++u) {
          const bf16x8 wf = *(LAS bf16x8*)(B + kk * 14336 + (n * 2 + u) * 1024);
          acc[n][u][0] = MFMA16(wf, a0, acc[n][u][0]);
          acc[n][u][1] = MFMA16(wf, a1, acc[n][u][1]);
        }
    }
    slot = slot == 2 ? 0 : slot + 1;
  }
  constexpr float C1 = -2.0f * 0.7978845608028654f * 1.4426950408889634f, C2 = C1 * 0.044715f;
#pragma unroll
  for (int m = 0; m < 2; ++m) {
    const float sc = rstd[m], c1s = C1 * sc, c2s = C2 * sc * sc * sc, is2 = __builtin_amdgcn_rcpf(sc * sc);
    char* gb = ws + WS_G + (size_t)(T0 + m) * (54 * 1024) + g * 256 + r * 16;
#pragma unroll
    for (int n = 0; n < 7; ++n) {
      float o[4];
#pragma unroll
      for (int e = 0; e < 4; ++e) {
        const float a = acc[n][0][m][e], b = acc[n][1][m][e];
        const float z = a * __builtin_fmaf(a * a, c2s, c1s);
        const float ex = __builtin_amdgcn_exp2f(z);
        o[e] = (a * b) * __builtin_amdgcn_rcpf(__builtin_fmaf(ex, is2, is2));
      }
      const int f = t0 + n;
      u32x2 w; w.x = cvtpk(o[0], o[1]); w.y = cvtpk(o[2], o[3]);
      if (n < ntile) *(u32x2*)(gb + (f >> 1) * 1024 + (f & 1) * 8) = w;
    }
  }
}

extern "C" void kernel_launch(void* const* d_in, const int* in_sizes, int n_in, void* d_out, int out_size, void* d_ws, size_t ws_size,
                              hipStream_t stream) {
  const unsigned short* x = (const unsigned short*)d_in[0];
  const unsigned short* wqkv = (const unsigned short*)d_in[1];
  const unsigned short* wo = (const unsigned short*)d_in[2];
  const unsigned short* wg = (const unsigned short*)d_in[4];
  const unsigned short* wu = (const unsigned short*)d_in[5];
  const unsigned short* wd = (const unsigned short*)d_in[6];
  unsigned short* out = (unsigned short*)d_out;
  char* ws = (char*)d_ws;
  (void)hipFuncSetAttribute((const void*)k1_qkv, hipFuncAttributeMaxDynamicSharedMemorySize, K1_LDS);
  (void)hipFuncSetAttribute((const void*)k2_attn, hipFuncAttributeMaxDynamicSharedMemorySize, K2_LDS);
  (void)hipFuncSetAttribute((const void*)k_skinny<0>, hipFuncAttributeMaxDynamicSharedMemorySize, KS_LDS);
  (void)hipFuncSetAttribute((const void*)k_skinny<1>, hipFuncAttributeMaxDynamicSharedMemorySize, KS_LDS);
  (void)hipFuncSetAttribute((const void*)k4_gateup, hipFuncAttributeMaxDynamicSharedMemorySize, K4_LDS);
  k1_qkv<<<256, 256, K1_LDS, stream>>>(x, wqkv, ws);
  k2_attn<<<256, 512, K2_LDS, stream>>>(wo, wg, wu, wd, ws);
  k_skinny<0><<<256, 256, KS_LDS, stream>>>(x, out, ws);
  k4_gateup<<<256, 256, K4_LDS, stream>>>(ws);
  k_skinny<1><<<256, 256, KS_LDS, stream>>>(x, out, ws);
}
```

```cpp
#include <hip/hip_runtime.h>
#include <hip/hip_bf16.h>
#include <stdint.h>

#define LAS __attribute__((address_space(3)))
#define GAS __attribute__((address_space(1)))
typedef short bf16x8 __attribute__((ext_vector_type(8)));
typedef short s16x4 __attribute__((ext_vector_type(4)));
typedef __bf16 bf16x2 __attribute__((ext_vector_type(2)));
typedef float f32x2 __attribute__((ext_vector_type(2)));
typedef float f32x4 __attribute__((ext_vector_type(4)));
typedef float f32x16 __attribute__((ext_vector_type(16)));
typedef unsigned u32x2 __attribute__((ext_vector_type(2)));
typedef unsigned u32x4 __attribute__((ext_vector_type(4)));
typedef unsigned long long u64;
typedef unsigned short u16;
typedef LAS char* lptr;

constexpr int D_ = 640;
constexpr float LN_EPS = 1e-5f;
constexpr float QSCALE = 0.22360679774997896f * 1.4426950408889634f;

constexpr unsigned WS_FLAG = 0;
constexpr unsigned F_Q = 0, F_RT = 256, F_O = 512, F_H = 768, F_G = 1024, F_EPOCH = 1280;
constexpr unsigned WS_QKV = 262144;
constexpr unsigned WS_O   = WS_QKV + 3 * 32 * 128 * 640;
constexpr unsigned WS_H   = WS_O + 128 * 20 * 1024;
constexpr unsigned WS_G   = WS_H + 128 * 20 * 1024;
constexpr unsigned WS_WO  = WS_G + 128 * 54 * 1024;
constexpr unsigned WS_WD  = WS_WO + 8 * 20 * 5 * 1024;
constexpr unsigned WS_WGU = WS_WD + 8 * 54 * 5 * 1024;
constexpr unsigned WS_HST = WS_WGU + 20 * 108 * 2 * 1024 + 4096;
constexpr unsigned WS_CS  = WS_HST + 128 * 8 * 16 * 8;
constexpr unsigned WS_END = WS_CS + 4 * 2 * 1728 * 4 + 4096;

constexpr int LDS_BYTES = 163840;
constexpr int L_MISC = 161792;
constexpr int L_CNT = LDS_BYTES - 64;
constexpr int P1_STAGE = 32768, L_RT = 3 * P1_STAGE, L_RTCS = 159744;
constexpr int A_K = 0, A_V = 20480, A_Q = 40960, A_ONE = 51200, L_WO = 57344;
constexpr int P4_W = 28672, P4_A0 = 3 * P4_W, P4_A = 16384;
constexpr int P5_STAGE = 20480;

#define STAMP(i) do {} while (0)

__device__ __forceinline__ unsigned cvtpk(float lo, float hi) { return __builtin_bit_cast(unsigned, __builtin_convertvector(f32x2{lo, hi}, bf16x2)); }
__device__ __forceinline__ float bflo(unsigned v) { return __uint_as_float(v << 16); }
__device__ __forceinline__ float bfhi(unsigned v) { return __uint_as_float(v & 0xffff0000u); }
__device__ __forceinline__ s16x4 trread(lptr p) { return __builtin_amdgcn_ds_read_tr16_b64_v4i16((LAS s16x4*)p); }
__device__ __forceinline__ bf16x8 cat8(s16x4 a, s16x4 b) { return __builtin_shufflevector(a, b, 0, 1, 2, 3, 4, 5, 6, 7); }
__device__ __forceinline__ bf16x8 lds16(lptr p) { return *(LAS bf16x8*)p; }
#define MFMA16(a, b, c) __builtin_amdgcn_mfma_f32_16x16x32_bf16(a, b, c, 0, 0, 0)
#define MFMA32(a, b, c) __builtin_amdgcn_mfma_f32_32x32x16_bf16(a, b, c, 0, 0, 0)
#define MFMA32K8(a, b, c) __builtin_amdgcn_mfma_f32_32x32x8bf16_1k(a, b, c, 0, 0, 0)
#define SB() __builtin_amdgcn_sched_barrier(0)
#define SGB(mask, n) __builtin_amdgcn_sched_group_barrier(mask, n, 0)
__device__ __forceinline__ unsigned rfl(unsigned v) { return __builtin_amdgcn_readfirstlane(v); }
__device__ __forceinline__ float shx(float v, int m) { return __shfl_xor(v, m); }
__device__ __forceinline__ u32x4 mkdesc(const void* p, unsigned bytes) { const u64 a = (u64)p; return u32x4{(unsigned)a, (unsigned)(a >> 32) & 0xffffu, bytes, 0x00020000u}; }
template <int N> __device__ __forceinline__ void waitv() { asm volatile("s_waitcnt vmcnt(%0)" :: "i"(N) : "memory"); }
template <int N> __device__ __forceinline__ void waitv_bar() { SB(); asm volatile("s_waitcnt vmcnt(%0) lgkmcnt(0)\n\ts_barrier" :: "i"(N) : "memory"); SB(); }
__device__ __forceinline__ void bar_l() { SB(); asm volatile("s_waitcnt lgkmcnt(0)\n\ts_barrier" ::: "memory"); SB(); }

#define DMA1(o, sc) "buffer_load_dwordx4 %0, %1, %2 offen offset:" #o sc " lds\n\t"
template <int N, bool SC1> __device__ __forceinline__ void dma_run(unsigned voff, u32x4 d, unsigned soff, unsigned lds) {
#define DMA_ASM(body) asm volatile("s_mov_b32 m0, %3\n\ts_nop 0\n\t" body :: "v"(voff), "s"(d), "s"(soff), "s"(lds) : "memory")
  if constexpr (SC1) {
    if constexpr (N == 1) DMA_ASM(DMA1(0, " sc1"));
    if constexpr (N == 2) DMA_ASM(DMA1(0, " sc1") DMA1(1024, " sc1"));
    if constexpr (N == 3) DMA_ASM(DMA1(0, " sc1") DMA1(1024, " sc1") DMA1(2048, " sc1"));
    if constexpr (N == 4) DMA_ASM(DMA1(0, " sc1") DMA1(1024, " sc1") DMA1(2048, " sc1") DMA1(3072, " sc1"));
  } else {
    if constexpr (N == 1) DMA_ASM(DMA1(0, ""));
    if constexpr (N == 2) DMA_ASM(DMA1(0, "") DMA1(1024, ""));
    if constexpr (N == 3) DMA_ASM(DMA1(0, "") DMA1(1024, "") DMA1(2048, ""));
    if constexpr (N == 4) DMA_ASM(DMA1(0, "") DMA1(1024, "") DMA1(2048, "") DMA1(3072, ""));
  }
#undef DMA_ASM
}
__device__ __forceinline__ void dma4v(const unsigned (&v)[4], u32x4 d, unsigned soff, unsigned lds) {
  asm volatile("s_mov_b32 m0, %6\n\ts_nop 0\n\t"
               "buffer_load_dwordx4 %0, %4, %5 offen lds\n\t"
               "buffer_load_dwordx4 %1, %4, %5 offen offset:1024 lds\n\t"
               "buffer_load_dwordx4 %2, %4, %5 offen offset:2048 lds\n\t"
               "buffer_load_dwordx4 %3, %4, %5 offen offset:3072 lds"
               :: "v"(v[0]), "v"(v[1]), "v"(v[2]), "v"(v[3]), "s"(d), "s"(soff), "s"(lds) : "memory");
}

typedef __amdgpu_buffer_rsrc_t rsrc_t;
__device__ __forceinline__ void st8(rsrc_t r, unsigned voff, unsigned soff, u32x2 v) { __builtin_amdgcn_raw_buffer_store_b64(v, r, voff, soff, 16); }
__device__ __forceinline__ void st16(rsrc_t r, unsigned voff, unsigned soff, u32x4 v) { __builtin_amdgcn_raw_buffer_store_b128(v, r, voff, soff, 16); }
__device__ __forceinline__ void st4(rsrc_t r, unsigned voff, unsigned soff, unsigned v) { __builtin_amdgcn_raw_buffer_store_b32(v, r, voff, soff, 16); }
__device__ __forceinline__ u32x4 ld16(rsrc_t r, unsigned voff, unsigned soff) { return __builtin_amdgcn_raw_buffer_load_b128(r, voff, soff, 16); }
__device__ __forceinline__ u32x2 ld8(rsrc_t r, unsigned voff, unsigned soff) { return __builtin_amdgcn_raw_buffer_load_b64(r, voff, soff, 16); }
__device__ __forceinline__ unsigned ld4(rsrc_t r, unsigned voff, unsigned soff) { return __builtin_amdgcn_raw_buffer_load_b32(r, voff, soff, 16); }

__device__ __forceinline__ u32x2 flagval(unsigned tag) { return u32x2{tag, tag * 0x9E3779B1u + 0x7F4A7C15u}; }
__device__ __forceinline__ void poll_flags(rsrc_t r, int fidx, unsigned tag) {
  const u32x2 want = flagval(tag);
  const unsigned off = (unsigned)(fidx < 0 ? 0 : fidx) * 128u;
#pragma unroll 1
  for (int spin = 0; spin < (1 << 16); ++spin) {
    const u32x2 v = ld8(r, off, WS_FLAG);
    if (__all(fidx < 0 || (v.x == want.x && v.y == want.y))) break;
    __builtin_amdgcn_s_sleep(1);
  }
  __builtin_amdgcn_fence(__ATOMIC_ACQUIRE, "wavefront");
  asm volatile("" ::: "memory");
}
__device__ __forceinline__ bool arrive_last(lptr L, int cnt, unsigned n) {
  asm volatile("s_waitcnt vmcnt(0) lgkmcnt(0)" ::: "memory");
  unsigned old = 0;
  if ((threadIdx.x & 63) == 0) old = __hip_atomic_fetch_add((LAS unsigned*)(L + L_CNT + 4 * cnt), 1u, __ATOMIC_RELAXED, __HIP_MEMORY_SCOPE_WORKGROUP);
  old = rfl(old);
  asm volatile("" ::: "memory");
  return old == n - 1;
}
__device__ __forceinline__ void group_sync(lptr L, int cnt, unsigned n) {
  asm volatile("s_waitcnt lgkmcnt(0)" ::: "memory");
  if ((threadIdx.x & 63) == 0) __hip_atomic_fetch_add((LAS unsigned*)(L + L_CNT + 4 * cnt), 1u, __ATOMIC_RELAXED, __HIP_MEMORY_SCOPE_WORKGROUP);
#pragma unroll 1
  for (int spin = 0; spin < (1 << 22); ++spin) { if (rfl(*(volatile LAS unsigned*)(L + L_CNT + 4 * cnt)) >= n) break; }
  asm volatile("" ::: "memory");
}
__device__ __forceinline__ void raise_flag(rsrc_t r, unsigned fidx, unsigned tag) { if ((threadIdx.x & 63) == 0) st8(r, 0, WS_FLAG + fidx * 128u, flagval(tag)); }
__device__ __forceinline__ void lds_signal(lptr L, int cnt) { asm volatile("" ::: "memory"); if ((threadIdx.x & 63) == 0) *(volatile LAS unsigned*)(L + L_CNT + 4 * cnt) = 1u; }
__device__ __forceinline__ void lds_wait(lptr L, int cnt) {
#pragma unroll 1
  for (int spin = 0; spin < (1 << 22); ++spin) { if (rfl(*(volatile LAS unsigned*)(L + L_CNT + 4 * cnt))) break; __builtin_amdgcn_s_sleep(2); }
  asm volatile("" ::: "memory");
}

__device__ __forceinline__ u32x4 pair16(u32x2 a, u32x2 b) {
  const auto x = __builtin_amdgcn_permlane16_swap(a.x, b.x, false, false), y = __builtin_amdgcn_permlane16_swap(a.y, b.y, false, false);
  return u32x4{x[0], y[0], x[1], y[1]};
}
__device__ __forceinline__ void retile_read(lptr u, int lane, u32x4 (&f)[4]) {
  const lptr rp = u + (8 * (lane >> 4) + ((lane & 15) >> 2)) * 128 + (lane & 3) * 8;
#pragma unroll
  for (int n = 0; n < 4; ++n) f[n] = __builtin_bit_cast(u32x4, cat8(trread(rp + n * 32), trread(rp + n * 32 + 512)));
}
__device__ __forceinline__ void colsum_out(const u32x4 (&f)[4], lptr dst, int lane) {
#pragma unroll
  for (int n = 0; n < 4; ++n) {
    float c = ((bflo(f[n].x) + bfhi(f[n].x)) + (bflo(f[n].y) + bfhi(f[n].y))) + ((bflo(f[n].z) + bfhi(f[n].z)) + (bflo(f[n].w) + bfhi(f[n].w)));
    c += shx(c, 16); c += shx(c, 32);
    if (lane < 16) *(LAS float*)(dst + (n * 16 + lane) * 4) = c;
  }
}

__global__ __launch_bounds__(512) __attribute__((amdgpu_waves_per_eu(2, 2)))
void mega(const u16* __restrict__ x, const u16* __restrict__ wqkv, const u16* __restrict__ wo, const u16* __restrict__ wg, const u16* __restrict__ wu,
          const u16* __restrict__ wd, u16* __restrict__ out, char* __restrict__ ws) {
  extern __shared__ __attribute__((aligned(16))) char smem[];
  const lptr L = (lptr)smem;
  const unsigned lds0 = (unsigned)(uintptr_t)L;
  const int bid = blockIdx.x, xcd = bid & 7, idx = bid >> 3;
  const int tid = threadIdx.x, lane = tid & 63, wid = rfl(tid >> 6), w4 = wid & 3;
  const bool isC = wid < 4;
  const int r = lane & 15, g = lane >> 4;
  const unsigned vo = lane * 16;
  const rsrc_t rws = __builtin_amdgcn_make_buffer_rsrc(ws, 0, WS_END, 0x00020000);
  const u32x4 dws = mkdesc(ws, WS_END);
  STAMP(0);
  const unsigned epoch_v = ld4(rws, 0, WS_FLAG + F_EPOCH * 128);
  if (tid < 16) *(LAS unsigned*)(L + L_CNT + 4 * tid) = 0u;
  unsigned tag;
  u32x4 rtf[2][4];
  unsigned rtd[2][4];
#pragma unroll
  for (int i = 0; i < 4; ++i) { rtf[0][i] = u32x4{0u, 0u, 0u, 0u}; rtf[1][i] = rtf[0][i]; rtd[0][i] = 0u; rtd[1][i] = 0u; }
  int rid;

  const int tm = 4 * (xcd >> 1) + (idx & 3), tn = 8 * (xcd & 1) + (idx >> 2);
  if (tn < 15) {
    const int aid = (xcd >> 1) * 60 + (xcd & 1) * 32 + idx;
    rid = aid;
    if (!isC) {
      unsigned voffX[4], voffW[4];
#pragma unroll
      for (int i = 0; i < 4; ++i) {
        const int p = w4 * 4 + i;
        voffX[i] = (unsigned)((8 * p + (lane >> 3)) * 1280 + (((lane & 7) ^ (lane >> 3)) * 16) - 1024 * i);
        const int f = (lane >> 4) | (((p >> 1) & 1) << 2), p16 = lane & 15;
        voffW[i] = (unsigned)((4 * p + (lane >> 4)) * 3840 + (((((p16 >> 1) ^ f) << 1) | (p16 & 1)) * 16) - 1024 * i);
      }
      const u32x4 dx = mkdesc(x, 2048 * 1280), dw = mkdesc(wqkv, 640 * 3840);
      const unsigned xs0 = tm * 128 * 1280, ws0 = tn * 256, ld0 = lds0 + w4 * 4096;
      dma4v(voffX, dx, xs0, ld0); dma4v(voffW, dw, ws0, ld0 + 16384);
      int slot = 1;
#pragma unroll 1
      for (int st = 1; st < 10; ++st) {
        dma4v(voffX, dx, xs0 + st * 128, ld0 + slot * P1_STAGE); dma4v(voffW, dw, ws0 + st * (64 * 3840), ld0 + slot * P1_STAGE + 16384);
        slot = slot == 2 ? 0 : slot + 1;
        waitv_bar<8>();
      }
      const bool gu_ok = aid < 216, d_ok = aid < 180;
      const int cb = (gu_ok ? aid : 0) >> 2, isup = cb >= 27, cbb = cb - 27 * isup;
      const u32x4 dgu = mkdesc(isup ? wu : wg, 640 * 3456), dd = mkdesc(wd, 1728 * 1280);
      unsigned vgu[4], vd[4];
#pragma unroll
      for (int i = 0; i < 4; ++i) { vgu[i] = (8 * i + (lane >> 3)) * 3456 + (lane & 7) * 16 - 1024 * i; vd[i] = (8 * i + (lane >> 3)) * 1280 + (lane & 7) * 16 - 1024 * i; }
      const int sA = 5 * (aid & 3) + w4, sB = 5 * (aid & 3) + 4;
      const int ud = (d_ok ? aid : 0) * 3 + (w4 > 0 ? w4 - 1 : 0), dcb = ud / 54, dsx = ud - 54 * dcb;
      dma4v(vgu, dgu, sA * (32 * 3456) + cbb * 128, lds0 + L_RT + w4 * 4096);
      if (w4 == 0) dma4v(vgu, dgu, sB * (32 * 3456) + cbb * 128, lds0 + L_RT + 16384);
      else dma4v(vd, dd, dsx * (32 * 1280) + dcb * 128, lds0 + L_RT + 16384 + w4 * 4096);
#pragma unroll
      for (int n = 0; n < 4; ++n) {
        if (gu_ok) rtd[0][n] = WS_WGU + ((sA * 108 + 4 * cbb + n) * 2 + isup) * 1024;
        const int t = 4 * dcb + n, sl = t / 5;
        if (w4 == 0) { if (gu_ok) rtd[1][n] = WS_WGU + ((sB * 108 + 4 * cbb + n) * 2 + isup) * 1024; }
        else if (d_ok) rtd[1][n] = WS_WD + ((sl * 54 + dsx) * 5 + (t - 5 * sl)) * 1024;
      }
      waitv_bar<8>();
      bar_l();
      waitv<0>();
      retile_read(L + L_RT + w4 * 4096, lane, rtf[0]);
      retile_read(L + L_RT + 16384 + w4 * 4096, lane, rtf[1]);
      colsum_out(rtf[0], L + L_RTCS + w4 * 256, lane);
      if (w4 == 0) colsum_out(rtf[1], L + L_RTCS + 1024, lane);
      tag = rfl(epoch_v) + 1u;
    } else {
      const int wr = w4 >> 1, wc = w4 & 1;
      const int xo0 = (64 * wr + r) * 128 + ((g ^ (r & 7)) * 16);
      const int xoA = xo0 + 4096 * wc, xoB = xo0 + 4096 * (1 - wc);
      int wo_[4];
      { const int q = r >> 2, p = lane & 3, f = q | ((g & 1) << 2);
#pragma unroll
        for (int n = 0; n < 4; ++n) wo_[n] = 16384 + (8 * g + q) * 256 + (((4 * wc + (n ^ (2 * wr))) ^ f) * 32) + p * 8; }
      f32x4 acc[4][4], ssum[2], gram[2], csum[2];
#pragma unroll
      for (int n = 0; n < 4; ++n)
#pragma unroll
        for (int m = 0; m < 4; ++m) acc[n][m] = f32x4{0.f, 0.f, 0.f, 0.f};
#pragma unroll
      for (int i = 0; i < 2; ++i) { ssum[i] = f32x4{0.f, 0.f, 0.f, 0.f}; gram[i] = ssum[i]; csum[i] = ssum[i]; }
      const bf16x8 ones = {0x3F80, 0x3F80, 0x3F80, 0x3F80, 0x3F80, 0x3F80, 0x3F80, 0x3F80};
      bf16x8 xa[4], wa[4], xb[4], wb[4];
#define P1_READ(xf, wf, B, kk) do { \
        xf[0] = lds16(B + (xoA ^ (64 * kk))); xf[1] = lds16(B + (xoA ^ (64 * kk)) + 2048); \
        wf[0] = cat8(trread(B + wo_[0] + 8192 * kk), trread(B + wo_[0] + 8192 * kk + 1024)); \
        xf[2] = lds16(B + (xoB ^ (64 * kk))); xf[3] = lds16(B + (xoB ^ (64 * kk)) + 2048); \
        _Pragma("unroll") for (int n = 1; n < 4; ++n) wf[n] = cat8(trread(B + wo_[n] + 8192 * kk), trread(B + wo_[n] + 8192 * kk + 1024)); } while (0)
#define P1_MMA(xf, wf, nrd) do { \
        _Pragma("unroll") for (int n = 0; n < 4; ++n) _Pragma("unroll") for (int m = 0; m < 4; ++m) acc[n][m] = MFMA16(wf[n], xf[m], acc[n][m]); \
        _Pragma("unroll") for (int i = 0; i < 2; ++i) { ssum[i] = MFMA16(ones, xf[i], ssum[i]); gram[i] = MFMA16(xf[i], xf[i], gram[i]); csum[i] = MFMA16(wf[i], ones, csum[i]); } \
        _Pragma("unroll") for (int i = 0; i < nrd; ++i) { SGB(0x008, 1); SGB(0x100, 1); } SGB(0x008, 22 - nrd); } while (0)
      bar_l();
      STAMP(17);
      P1_READ(xa, wa, L, 0);
      SB();
      int slot = 0;
#pragma unroll 1
      for (int st = 0; st < 10; ++st) {
        const lptr B = L + slot * P1_STAGE;
        slot = slot == 2 ? 0 : slot + 1;
        const lptr Bn = L + slot * P1_STAGE;
        P1_READ(xb, wb, B, 1);
        P1_MMA(xa, wa, 12);
        bar_l();
        P1_READ(xa, wa, Bn, 0);
        P1_MMA(xb, wb, 12);
        SB();
      }
      STAMP(1);
      LAS f32x2* st_tok = (LAS f32x2*)(L + L_MISC);
      LAS float* st_col = (LAS float*)(L + L_MISC + 1024);
#pragma unroll
      for (int i = 0; i < 2; ++i) {
        const int j = r & 3;
        const float sq = j == 0 ? gram[i][0] : j == 1 ? gram[i][1] : j == 2 ? gram[i][2] : gram[i][3];
        const float mean = ssum[i][0] * (1.0f / D_);
        const float var = fmaxf(sq * (1.0f / D_) - mean * mean, 0.f);
        if (g == (r >> 2)) st_tok[64 * wr + 16 * (2 * wc + i) + r] = f32x2{mean, __builtin_amdgcn_rsqf(var + LN_EPS)};
        if (r == 0) *(LAS f32x4*)(st_col + 64 * wc + 16 * (2 * wr + i) + 4 * g) = csum[i];
      }
      group_sync(L, 6, 4);
      const int tensor = tn / 5;
      const float osc = tensor == 0 ? QSCALE : 1.0f;
      f32x2 ts[4];
#pragma unroll
      for (int m = 0; m < 4; ++m) { ts[m] = st_tok[64 * wr + 16 * (m ^ (2 * wc)) + r]; ts[m].y *= osc; }
      const unsigned so = WS_QKV + (tensor * 32 * 64 + 4 * tm + 2 * wr) * 1280;
#pragma unroll
      for (int n = 0; n < 4; ++n) {
        const int np = n ^ (2 * wr);
        const f32x4 cs = *(LAS f32x4*)(st_col + 64 * wc + 16 * np + 4 * g);
        const int c4 = (tn - 5 * tensor) * 32 + 16 * wc + 4 * np + g;
        const int head = c4 / 5, dg = c4 - 5 * head;
        const unsigned dv = head * (64 * 1280) + dg * 256 + r * 16;
#pragma unroll
        for (int m = 0; m < 4; m += 2) {
          const f32x4 v = (acc[n][m] - ts[m].x * cs) * ts[m].y, w = (acc[n][m + 1] - ts[m + 1].x * cs) * ts[m + 1].y;
          st16(rws, dv, so + ((m >> 1) ^ wc) * 1280, u32x4{cvtpk(v[0], v[1]), cvtpk(v[2], v[3]), cvtpk(w[0], w[1]), cvtpk(w[2], w[3])});
        }
      }
      tag = rfl(epoch_v) + 1u;
      STAMP(2);
      if (arrive_last(L, 0, 4)) raise_flag(rws, F_Q + tm * 16 + tn, tag);
    }
  } else {
    const int ii = (xcd >> 1) * 4 + (idx - 28);
    rid = 240 + ii;
    const u32x4 dwo = mkdesc(wo, 640 * 1280);
    const int u0 = ii * 8 + wid;
#pragma unroll
    for (int k = 0; k < 2; ++k) {
      const int u = (k == 1 && u0 + 128 < 200) ? u0 + 128 : u0, s0 = u / 10, c0 = u - 10 * s0, j = lane >> 3;
      unsigned vd[4];
#pragma unroll
      for (int i = 0; i < 4; ++i) {
        const int ch = 4 * s0 + i;
        const int row = ch < 64 ? 20 * (ch >> 1) + 4 * (ch & 1) + (j & 3) + 8 * (j >> 2) : 20 * (2 * (ch - 64) + (j >> 2)) + 16 + (j & 3);
        vd[i] = row * 1280 + (lane & 7) * 16 - 1024 * i;
        const int t = 4 * c0 + i, sl = t / 5;
        rtd[k][i] = WS_WO + ((sl * 20 + s0) * 5 + (t - 5 * sl)) * 1024;
      }
      dma4v(vd, dwo, c0 * 128, lds0 + L_RT - k * 32768 + wid * 4096);
    }
    waitv_bar<0>();
    retile_read(L + L_RT + wid * 4096, lane, rtf[0]);
    retile_read(L + L_RT - 32768 + wid * 4096, lane, rtf[1]);
#pragma unroll
    for (int k = 0; k < 2; ++k)
#pragma unroll
      for (int n = 0; n < 4; ++n) { st16(rws, vo, rtd[k][n], rtf[k][n]); rtd[k][n] = 0u; }
    tag = rfl(epoch_v) + 1u;
    if (arrive_last(L, 5, 8)) raise_flag(rws, F_RT + rid, tag);
  }
  STAMP(3);

  const int head = 4 * xcd + (idx >> 3), qb = idx & 7;
  const int rt = 4 * xcd + (idx & 3), ct = idx >> 2;
  if (isC) {
    if (wid == 0) {
      int f = -1;
      if (lane < 24) {
        const int tmi = lane / 6, wh = lane - 6 * tmi, tmm = 2 * qb - 2 + tmi, ten = wh >> 1;
        const int tnn = 5 * ten + ((20 * head + ((wh & 1) ? 19 : 0)) >> 7);
        if (tmm >= 0 && (ten > 0 || tmi >= 2)) f = F_Q + tmm * 16 + tnn;
      } else if (lane < 40) f = F_RT + 240 + (lane - 24);
      poll_flags(rws, f, tag);
      lds_signal(L, 8);
    } else lds_wait(L, 8);
    STAMP(4);
    const unsigned hb = WS_QKV + (head * 64 + 8 * qb) * 1280, prev = qb > 0 ? 10240 : 0;
    const unsigned src = hb + ((w4 >> 1) + 1) * (32 * 128 * 640) - ((w4 & 1) ? 0 : prev);
    const unsigned dst = lds0 + w4 * 10240;
    if (w4 < 2) dma_run<3, true>(vo, dws, hb + w4 * 3072, lds0 + A_Q + w4 * 3072);
    else dma_run<2, true>(vo, dws, hb + 6144 + (w4 - 2) * 2048, lds0 + A_Q + 6144 + (w4 - 2) * 2048);
    dma_run<4, true>(vo, dws, src, dst);
    dma_run<4, true>(vo, dws, src + 4096, dst + 4096);
    dma_run<2, true>(vo, dws, src + 8192, dst + 8192);
  } else if (wid == 4 && lane < 32) *(LAS u32x2*)(L + A_ONE + lane * 8) = u32x2{0x00003F80u, 0u};
  waitv_bar<0>();
  STAMP(5);
  if (!isC && rid < 240) {
#pragma unroll
    for (int k = 0; k < 2; ++k)
#pragma unroll
      for (int n = 0; n < 4; ++n) if (rtd[k][n]) st16(rws, vo, rtd[k][n], rtf[k][n]);
    if (w4 == 0 && rid < 216) {
      float t = 0.f;
#pragma unroll
      for (int w = 0; w < 5; ++w) t += *(LAS float*)(L + L_RTCS + (w * 64 + lane) * 4);
      st4(rws, lane * 4, WS_CS + ((rid & 3) * 3456 + (rid >> 2) * 64) * 4, __float_as_uint(t));
    }
  }
  {
    const int c = lane & 31, hh = lane >> 5;
    const int nskip = qb == 0 ? 8 - wid : 0;
    const unsigned wos = WS_WO + ct * 102400 + wid * 12288, wod = lds0 + L_WO + wid * 12288;
    bf16x8 q1; s16x4 q2;
    { const lptr qp = L + A_Q + wid * 1280 + (c & 15) * 16 + (c >> 4) * 8;
      q1 = cat8(*(LAS s16x4*)(qp + 512 * hh), *(LAS s16x4*)(qp + 512 * hh + 256));
      q2 = *(LAS s16x4*)(qp + 1024); if (hh) q2 = s16x4{0, 0, 0, 0}; }
    const lptr kp = L + A_K + wid * 1280 + (c & 15) * 16 + (c >> 4) * 8;
    f32x16 s[9];
    const f32x16 zero16 = {0.f,0.f,0.f,0.f,0.f,0.f,0.f,0.f,0.f,0.f,0.f,0.f,0.f,0.f,0.f,0.f};
#pragma unroll
    for (int jj = 0; jj < 9; ++jj) {
      const int jb = jj == 0 ? 0 : jj == 1 ? 8 : jj - 1;
      const bf16x8 k1 = cat8(*(LAS s16x4*)(kp + jb * 1280 + 512 * hh), *(LAS s16x4*)(kp + jb * 1280 + 512 * hh + 256));
      const s16x4 k2 = *(LAS s16x4*)(kp + jb * 1280 + 1024);
      s[jb] = MFMA32(k1, q1, zero16);
      s[jb] = MFMA32K8(k2, q2, s[jb]);
    }
    if (!isC && rid < 240) { if (arrive_last(L, 1, 4)) raise_flag(rws, F_RT + rid, tag); }
    dma_run<4, true>(vo, dws, wos, wod);
    if (nskip > 0) {
#pragma unroll
      for (int jb = 0; jb < 8; ++jb)
        if (jb < nskip) {
#pragma unroll
          for (int e = 0; e < 16; ++e) s[jb][e] = -INFINITY;
        }
    }
#pragma unroll
    for (int e = 0; e < 16; ++e) { const int kk = (e & 3) + 8 * (e >> 2) + 4 * hh; s[0][e] = kk > c ? s[0][e] : s[8][e]; }
    float m0 = s[0][0], m1 = s[0][1];
#pragma unroll
    for (int jb = 0; jb < 8; ++jb)
#pragma unroll
      for (int e = (jb == 0 ? 2 : 0); e < 16; e += 4) {
        m0 = fmaxf(fmaxf(m0, s[jb][e]), s[jb][e + 1]);
        if (e + 2 < 16) m1 = fmaxf(fmaxf(m1, s[jb][e + 2]), s[jb][e + 3]);
      }
    float mx = fmaxf(m0, m1);
    { auto rr = __builtin_amdgcn_permlane32_swap(__float_as_uint(mx), __float_as_uint(mx), false, false);
      mx = fmaxf(__uint_as_float(rr[0]), __uint_as_float(rr[1])); }
    dma_run<4, true>(vo, dws, wos + 4096, wod + 4096);
    const int gq = r >> 2, gp = lane & 3, up16 = (lane >> 4) & 1;
    const bool vconst = up16 && gp > 0;
    const int vbase = vconst ? A_ONE + gq * 16 : A_V + wid * 1280 + (4 * up16 + gp) * 256 + (4 * hh + gq) * 16;
    const int vstep = vconst ? 0 : 1280;
    f32x16 o = zero16;
    auto pv = [&](const u32x4 pa, const u32x4 pb, int jb) {
      const lptr v0 = L + vbase + jb * vstep;
      const bf16x8 va = cat8(trread(v0), trread(v0 + 128)), vb = cat8(trread(v0 + 8), trread(v0 + 136));
      o = MFMA32(va, __builtin_bit_cast(bf16x8, pa), o);
      o = MFMA32(vb, __builtin_bit_cast(bf16x8, pb), o);
    };
#pragma unroll
    for (int jb = 0; jb < 8; ++jb) {
      float p[16];
#pragma unroll
      for (int e = 0; e < 16; ++e) p[e] = __builtin_amdgcn_exp2f(s[jb][e] - mx);
      u32x4 pa = {cvtpk(p[0], p[1]), cvtpk(p[2], p[3]), cvtpk(p[4], p[5]), cvtpk(p[6], p[7])};
      u32x4 pb = {cvtpk(p[8], p[9]), cvtpk(p[10], p[11]), cvtpk(p[12], p[13]), cvtpk(p[14], p[15])};
      if (jb == 0) {
        u32x4 ma, mb;
#pragma unroll
        for (int e = 0; e < 4; ++e) {
          const int ka = 2 * (e & 1) + 8 * (e >> 1) + 4 * hh, kb = ka + 16;
          ma[e] = (ka > c ? 0xffffu : 0u) | (ka + 1 > c ? 0xffff0000u : 0u);
          mb[e] = (kb > c ? 0xffffu : 0u) | (kb + 1 > c ? 0xffff0000u : 0u);
        }
        pv(pa & ma, pb & mb, 0);
        pv(pa & ~ma, pb & ~mb, 8);
      } else pv(pa, pb, jb);
      if (jb == 3) dma_run<4, true>(vo, dws, wos + 8192, wod + 8192);
      if (jb == 5 && isC) dma_run<1, true>(vo, dws, WS_WO + ct * 102400 + 98304 + wid * 1024, lds0 + L_WO + 98304 + wid * 1024);
    }
    const float rl = __builtin_amdgcn_rcpf(hh ? o[8] : o[12]);
    const unsigned ob = WS_O + (16 * qb + 2 * wid) * 20480, ov = (c >> 4) * 20480 + (c & 15) * 16;
    st16(rws, ov + hh * 256, ob + head * 512, u32x4{cvtpk(o[0] * rl, o[1] * rl), cvtpk(o[2] * rl, o[3] * rl), cvtpk(o[4] * rl, o[5] * rl), cvtpk(o[6] * rl, o[7] * rl)});
    if (hh == 0) st8(rws, ov, ob + 16384 + (head >> 1) * 256 + (head & 1) * 8, u32x2{cvtpk(o[8] * rl, o[9] * rl), cvtpk(o[10] * rl, o[11] * rl)});
    STAMP(6);
    if (arrive_last(L, 2, 8)) raise_flag(rws, F_O + qb * 32 + head, tag);
  }
  bar_l();
  STAMP(7);

  const int pn = 4 * (xcd >> 1) + (idx & 3), cgp = 8 * (xcd & 1) + (idx >> 2);
  const int t0 = cgp < 12 ? 7 * cgp : 6 * cgp + 12, ntile = cgp < 12 ? 7 : 6;
  const int T5 = 4 * rt + w4;
  u32x2 hreg[5];
  const unsigned wgs = WS_WGU + t0 * 2048 + (w4 >> 1) * (108 * 2048) + (w4 & 1) * 7168;
  if (isC) {
    u32x2 xv[5];
#pragma unroll
    for (int n = 0; n < 5; ++n) xv[n] = *(const u32x2*)(x + (size_t)(16 * T5 + r) * D_ + 80 * ct + 16 * n + 4 * g);
    if (wid == 0) { poll_flags(rws, lane < 32 ? (int)(F_O + (rt >> 2) * 32 + lane) : -1, tag); lds_signal(L, 9); }
    else lds_wait(L, 9);
    STAMP(8);
    u32x4 af[20];
#pragma unroll
    for (int s = 0; s < 20; ++s) af[s] = ld16(rws, vo + (s & 3) * 1024, WS_O + T5 * 20480 + (s >> 2) * 4096);
    SB();
    f32x4 acc[5];
#pragma unroll
    for (int n = 0; n < 5; ++n) acc[n] = f32x4{0.f, 0.f, 0.f, 0.f};
    const lptr B = L + L_WO + vo;
    bf16x8 wf[2][5];
#pragma unroll
    for (int n = 0; n < 5; ++n) wf[0][n] = lds16(B + n * 1024);
    SB();
#pragma unroll
    for (int s = 0; s < 20; ++s) {
      if (s + 1 < 20) {
#pragma unroll
        for (int n = 0; n < 5; ++n) wf[(s + 1) & 1][n] = lds16(B + ((s + 1) * 5 + n) * 1024);
      }
#pragma unroll
      for (int n = 0; n < 5; ++n) acc[n] = MFMA16(wf[s & 1][n], __builtin_bit_cast(bf16x8, af[s]), acc[n]);
      SB();
    }
    STAMP(9);
    float s1 = 0.f, s2 = 0.f;
#pragma unroll
    for (int n = 0; n < 5; ++n)
      hreg[n] = u32x2{cvtpk(acc[n][0] + bflo(xv[n].x), acc[n][1] + bfhi(xv[n].x)), cvtpk(acc[n][2] + bflo(xv[n].y), acc[n][3] + bfhi(xv[n].y))};
    { const unsigned hv = (g & 1) * 512 + (g >> 1) * 256 + r * 16, hsb = WS_H + T5 * 20480 + ct * 2560;
      st16(rws, hv, hsb, pair16(hreg[0], hreg[1]));
      st16(rws, hv, hsb + 1024, pair16(hreg[2], hreg[3]));
      const u32x4 last = pair16(hreg[4], hreg[4]);
      if (!(g & 1)) st16(rws, hv, hsb + 2048, last); }
#pragma unroll
    for (int n = 0; n < 5; ++n) {
      const float a = bflo(hreg[n].x), b = bfhi(hreg[n].x), c = bflo(hreg[n].y), d = bfhi(hreg[n].y);
      s1 += (a + b) + (c + d); s2 += (a * a + b * b) + (c * c + d * d);
    }
    s1 += shx(s1, 16); s2 += shx(s2, 16); s1 += shx(s1, 32); s2 += shx(s2, 32);
    if (g == 0) st8(rws, r * 8, WS_HST + (T5 * 8 + ct) * 128, u32x2{__float_as_uint(s1), __float_as_uint(s2)});
    if (arrive_last(L, 3, 4)) raise_flag(rws, F_H + rt * 8 + ct, tag);
  } else {
    int f = -1;
    if (lane < 24) { const int b = lane >> 2, isup = b >= 3, cb = (t0 >> 2) + b - 3 * isup; if (cb <= ((t0 + ntile - 1) >> 2)) f = F_RT + (cb + 27 * isup) * 4 + (lane & 3); }
    poll_flags(rws, f, tag);
#pragma unroll
    for (int st = 0; st < 2; ++st) {
      dma_run<4, true>(vo, dws, wgs + st * (2 * 108 * 2048), lds0 + st * P4_W + w4 * 7168);
      dma_run<3, true>(vo, dws, wgs + st * (2 * 108 * 2048) + 4096, lds0 + st * P4_W + w4 * 7168 + 4096);
    }
    const int cc = w4 * 64 + lane;
    if (cc < 224) {
      const unsigned o = (cc >= 112 ? 1728 - 112 : 0) + cc + t0 * 16;
      const float t = (__uint_as_float(ld4(rws, o * 4, WS_CS)) + __uint_as_float(ld4(rws, o * 4, WS_CS + 3456 * 4))) +
                      (__uint_as_float(ld4(rws, o * 4, WS_CS + 2 * 3456 * 4)) + __uint_as_float(ld4(rws, o * 4, WS_CS + 3 * 3456 * 4)));
      *(LAS float*)(L + L_MISC + cc * 4) = t;
    }
#pragma unroll
    for (int n = 0; n < 5; ++n) hreg[n] = u32x2{0u, 0u};
  }
  STAMP(10);
  bar_l();

  const int T4 = 8 * pn + 2 * w4;
  if (!isC) {
    dma_run<4, true>(vo, dws, wgs + 2 * (2 * 108 * 2048), lds0 + 2 * P4_W + w4 * 7168);
    dma_run<3, true>(vo, dws, wgs + 2 * (2 * 108 * 2048) + 4096, lds0 + 2 * P4_W + w4 * 7168 + 4096);
    poll_flags(rws, lane < 8 ? (int)(F_H + (2 * pn + (w4 >> 1)) * 8 + lane) : -1, tag);
    STAMP(11);
    const unsigned as = WS_H + T4 * 20480, ad = lds0 + P4_A0 + w4 * 4096;
#pragma unroll
    for (int st = 0; st < 3; ++st) { dma_run<2, true>(vo, dws, as + st * 2048, ad + st * P4_A); dma_run<2, true>(vo, dws, as + 20480 + st * 2048, ad + st * P4_A + 2048); }
    waitv_bar<8>();
    waitv_bar<4>();
    int slot = 0;
#pragma unroll 1
    for (int st = 3; st < 10; ++st) {
      dma_run<2, true>(vo, dws, as + st * 2048, ad + slot * P4_A); dma_run<2, true>(vo, dws, as + 20480 + st * 2048, ad + slot * P4_A + 2048);
      dma_run<4, true>(vo, dws, wgs + st * (2 * 108 * 2048), lds0 + slot * P4_W + w4 * 7168);
      dma_run<3, true>(vo, dws, wgs + st * (2 * 108 * 2048) + 4096, lds0 + slot * P4_W + w4 * 7168 + 4096);
      slot = slot == 2 ? 0 : slot + 1;
      waitv_bar<11>();
    }
    waitv_bar<0>();
    int f = -1;
    if (lane < 36) { const int b = lane / 18; f = F_RT + (((5 * ct) >> 2) + b) * 18 + (lane - 18 * b); }
    poll_flags(rws, f, tag);
    bar_l();
    STAMP(12);
    const unsigned wds = WS_WD + ct * (54 * 5120) + w4 * 5120, wdd = lds0 + w4 * 5120;
#pragma unroll 1
    for (int st = 0; st < 7; ++st) {
      if (st == 4) bar_l();
      dma_run<4, true>(vo, dws, wds + st * P5_STAGE, wdd + st * P5_STAGE); dma_run<1, true>(vo, dws, wds + st * P5_STAGE + 4096, wdd + st * P5_STAGE + 4096);
    }
    waitv_bar<30>();
    waitv_bar<25>();
#pragma unroll 1
    for (int st = 7; st < 13; ++st) {
      dma_run<4, true>(vo, dws, wds + st * P5_STAGE, wdd + (st - 7) * P5_STAGE); dma_run<1, true>(vo, dws, wds + st * P5_STAGE + 4096, wdd + (st - 7) * P5_STAGE + 4096);
      waitv_bar<25>();
    }
    { const unsigned s13 = WS_WD + ct * (54 * 5120) + 13 * P5_STAGE, d13 = lds0 + 6 * P5_STAGE;
      if (w4 < 2) dma_run<3, true>(vo, dws, s13 + w4 * 3072, d13 + w4 * 3072); else dma_run<2, true>(vo, dws, s13 + 6144 + (w4 - 2) * 2048, d13 + 6144 + (w4 - 2) * 2048); }
    waitv_bar<22>();
    waitv_bar<17>(); waitv_bar<12>(); waitv_bar<7>(); waitv_bar<2>(); waitv_bar<0>();
    if (bid == 0 && wid == 4) {
#pragma unroll 1
      for (int i = 0; i < 4; ++i) poll_flags(rws, F_RT + i * 64 + lane, tag);
      if (lane == 0) st4(rws, 0, WS_FLAG + F_EPOCH * 128, tag);
    }
  } else {
    f32x4 acc[7][2][2];
#pragma unroll
    for (int n = 0; n < 7; ++n)
#pragma unroll
      for (int u = 0; u < 2; ++u) { acc[n][u][0] = f32x4{0.f, 0.f, 0.f, 0.f}; acc[n][u][1] = acc[n][u][0]; }
    bf16x8 a0[2], a1[2], w0[7], w1[7];
#define P4_RW(wf, W, kk, half) do { _Pragma("unroll") for (int i = 0; i < 7; ++i) wf[i] = lds16(W + (kk) * 14336 + ((half) * 7 + i) * 1024); } while (0)
#define P4_RA(af, A, kk) do { af[0] = lds16(A + (kk) * 1024); af[1] = lds16(A + 2048 + (kk) * 1024); } while (0)
#define P4_MMA(wf, af, half, nrd) do { _Pragma("unroll") for (int i = 0; i < 7; ++i) { const int t = (half) * 7 + i; \
        acc[t >> 1][t & 1][0] = MFMA16(wf[i], af[0], acc[t >> 1][t & 1][0]); acc[t >> 1][t & 1][1] = MFMA16(wf[i], af[1], acc[t >> 1][t & 1][1]); } \
        _Pragma("unroll") for (int i = 0; i < nrd; ++i) { SGB(0x008, 1); SGB(0x100, 1); } SGB(0x008, 14 - nrd); SB(); } while (0)
    bar_l();
    u32x2 hs[2][2];
#pragma unroll
    for (int m = 0; m < 2; ++m) { hs[m][0] = ld8(rws, lane * 8, WS_HST + (T4 + m) * 1024); hs[m][1] = ld8(rws, lane * 8, WS_HST + (T4 + m) * 1024 + 512); }
    STAMP(11);
    P4_RA(a0, L + P4_A0 + w4 * 4096 + vo, 0); P4_RW(w0, L + vo, 0, 0);
    SB();
    int slot = 0;
#pragma unroll 1
    for (int st = 0; st < 10; ++st) {
      const lptr W = L + slot * P4_W + vo, A = L + P4_A0 + slot * P4_A + w4 * 4096 + vo;
      slot = slot == 2 ? 0 : slot + 1;
      const lptr Wn = L + slot * P4_W + vo, An = L + P4_A0 + slot * P4_A + w4 * 4096 + vo;
      P4_RW(w1, W, 0, 1);                     P4_MMA(w0, a0, 0, 7);
      P4_RA(a1, A, 1); P4_RW(w0, W, 1, 0);    P4_MMA(w1, a0, 1, 9);
      P4_RW(w1, W, 1, 1);                     P4_MMA(w0, a1, 0, 7);
      bar_l();
      P4_RA(a0, An, 0); P4_RW(w0, Wn, 0, 0);  P4_MMA(w1, a1, 1, 9);
    }
    STAMP(12);
    constexpr float C1 = -2.0f * 0.7978845608028654f * 1.4426950408889634f, C2 = C1 * 0.044715f;
    u32x2 gq[2][7];
#pragma unroll
    for (int m = 0; m < 2; ++m) {
      float s1 = __uint_as_float(hs[m][0].x) + __uint_as_float(hs[m][1].x), s2 = __uint_as_float(hs[m][0].y) + __uint_as_float(hs[m][1].y);
      s1 += shx(s1, 16); s2 += shx(s2, 16); s1 += shx(s1, 32); s2 += shx(s2, 32);
      const float mean = s1 * (1.0f / D_), var = fmaxf(s2 * (1.0f / D_) - mean * mean, 0.f) + LN_EPS;
      const float sc = __builtin_amdgcn_rsqf(var), c1s = C1 * sc, c2s = C2 * sc * sc * sc;
#pragma unroll
      for (int n = 0; n < 7; ++n) {
        const f32x4 cg = *(LAS f32x4*)(L + L_MISC + (n * 16 + 4 * g) * 4), cu = *(LAS f32x4*)(L + L_MISC + (112 + n * 16 + 4 * g) * 4);
        const f32x4 a = acc[n][0][m] - mean * cg, b = acc[n][1][m] - mean * cu;
        const f32x4 z = a * (a * a * c2s + c1s);
        f32x4 ex;
#pragma unroll
        for (int e = 0; e < 4; ++e) ex[e] = __builtin_amdgcn_exp2f(z[e]);
        const f32x4 dn = ex * var + var;
        f32x4 rc;
#pragma unroll
        for (int e = 0; e < 4; ++e) rc[e] = __builtin_amdgcn_rcpf(dn[e]);
        const f32x4 o = (a * b) * rc;
        gq[m][n] = u32x2{cvtpk(o[0], o[1]), cvtpk(o[2], o[3])};
      }
    }
#pragma unroll
    for (int n = 0; n < 7; ++n) {
      const u32x4 v = pair16(gq[0][n], gq[1][n]);
      if (n < ntile) st16(rws, (g & 1) * (54 * 1024) + (g >> 1) * 256 + r * 16, WS_G + T4 * (54 * 1024) + (t0 + n) * 512, v);
    }
    STAMP(13);
    waitv_bar<0>();
    if (wid == 0) raise_flag(rws, F_G + pn * 16 + cgp, tag);

    if (wid == 0) { poll_flags(rws, lane < 16 ? (int)(F_G + (rt >> 1) * 16 + lane) : -1, tag); lds_signal(L, 10); }
    else lds_wait(L, 10);
    STAMP(14);
    constexpr int WIN = 24;
    u32x4 gf[WIN];
    const unsigned gs = WS_G + T5 * (54 * 1024);
#pragma unroll
    for (int s = 0; s < WIN; ++s) gf[s] = ld16(rws, vo + (s & 3) * 1024, gs + (s >> 2) * 4096);
    SB();
    f32x4 ac[5];
#pragma unroll
    for (int n = 0; n < 5; ++n) ac[n] = f32x4{0.f, 0.f, 0.f, 0.f};
#pragma unroll
    for (int st = 0; st < 14; ++st) {
      bar_l();
      const lptr B = L + (st % 7) * P5_STAGE + vo;
      const int nk = st < 13 ? 4 : 2;
      bf16x8 wf[20];
#pragma unroll
      for (int i = 0; i < 5 * nk; ++i) wf[i] = lds16(B + i * 1024);

#pragma unroll
      for (int kk = 0; kk < nk; ++kk) {
        const int s = 4 * st + kk;
#pragma unroll
        for (int n = 0; n < 5; ++n) ac[n] = MFMA16(wf[kk * 5 + n], __builtin_bit_cast(bf16x8, gf[s % WIN]), ac[n]);
        if (s + WIN < 54) gf[s % WIN] = ld16(rws, vo + ((s + WIN) & 3) * 1024, gs + ((s + WIN) >> 2) * 4096);
      }
    }
    STAMP(15);
    u32x2 oq[5];
#pragma unroll
    for (int n = 0; n < 5; ++n) oq[n] = u32x2{cvtpk(ac[n][0] + bflo(hreg[n].x), ac[n][1] + bfhi(hreg[n].x)), cvtpk(ac[n][2] + bflo(hreg[n].y), ac[n][3] + bfhi(hreg[n].y))};
    u16* op = out + (size_t)(16 * T5 + r) * D_ + 80 * ct + 16 * (g & 1) + 8 * (g >> 1);
    *(u32x4*)op = pair16(oq[0], oq[1]);
    *(u32x4*)(op + 32) = pair16(oq[2], oq[3]);
    const u32x4 last = pair16(oq[4], oq[4]);
    if (!(g & 1)) *(u32x4*)(op + 64) = last;
    STAMP(16);
  }
}

extern "C" void kernel_launch(void* const* d_in, const int* in_sizes, int n_in, void* d_out, int out_size, void* d_ws, size_t ws_size,
                              hipStream_t stream) {
  (void)hipFuncSetAttribute((const void*)mega, hipFuncAttributeMaxDynamicSharedMemorySize, LDS_BYTES);
  mega<<<256, 512, LDS_BYTES, stream>>>((const u16*)d_in[0], (const u16*)d_in[1], (const u16*)d_in[2], (const u16*)d_in[4], (const u16*)d_in[5],
                                        (const u16*)d_in[6], (u16*)d_out, (char*)d_ws);
}
```

```cpp
#include <hip/hip_runtime.h>
#include <hip/hip_bf16.h>
#include <stdint.h>

#define LAS __attribute__((address_space(3)))
#define GAS __attribute__((address_space(1)))
typedef short bf16x8 __attribute__((ext_vector_type(8)));
typedef short s16x4 __attribute__((ext_vector_type(4)));
typedef __bf16 bf16x2 __attribute__((ext_vector_type(2)));
typedef float f32x2 __attribute__((ext_vector_type(2)));
typedef float f32x4 __attribute__((ext_vector_type(4)));
typedef float f32x16 __attribute__((ext_vector_type(16)));
typedef unsigned u32x2 __attribute__((ext_vector_type(2)));
typedef unsigned u32x4 __attribute__((ext_vector_type(4)));
typedef unsigned long long u64;
typedef unsigned short u16;
typedef LAS char* lptr;

constexpr int D_ = 640;
constexpr float LN_EPS = 1e-5f;
constexpr float QSCALE = 0.22360679774997896f * 1.4426950408889634f;

constexpr unsigned WS_FLAG = 0;
constexpr unsigned F_Q = 0, F_O = 256, F_H = 512, F_G = 768, F_RT = 1024, F_EPOCH = 1280;
constexpr unsigned WS_QKV = 262144;
constexpr unsigned WS_O   = WS_QKV + 3 * 32 * 128 * 640;
constexpr unsigned WS_H   = WS_O + 128 * 20 * 1024;
constexpr unsigned WS_G   = WS_H + 128 * 20 * 1024;
constexpr unsigned WS_WO  = WS_G + 128 * 54 * 1024;
constexpr unsigned WS_WD  = WS_WO + 8 * 20 * 5 * 1024;
constexpr unsigned WS_WGU = WS_WD + 8 * 54 * 5 * 1024;
constexpr unsigned WS_HST = WS_WGU + 20 * 108 * 2 * 1024 + 4096;
constexpr unsigned WS_CS  = WS_HST + 128 * 8 * 16 * 8;
constexpr unsigned WS_S1  = WS_CS + 4 * 2 * 1728 * 4 + 4096;
constexpr unsigned WS_C1  = WS_S1 + 2048 * 16;
constexpr unsigned WS_END = WS_C1 + 15 * 10 * 1024;

constexpr int LDS_BYTES = 163840;
constexpr int L_MISC = 161792;
constexpr int L_CNT = LDS_BYTES - 64;
constexpr int P1_STAGE = 32768, L_RT = 3 * P1_STAGE, L_GUCS = 159744;
constexpr int A_K = 0, A_V = 20480, A_Q = 40960, A_ONE = 51200, L_WO = 57344;
constexpr int P4_W = 28672, P4_A0 = 3 * P4_W, P4_A = 16384;
constexpr int P5_STAGE = 30720;

#define STAMP(i) do {} while (0)

#define ROT 8
#define TOUCH 0
#define WARM 1
#define RTOUCH 1
#define DTOUCH 1
#define GWIN 12
#define LPRIO 2
#define P5_PRE 5
#define P1_ROT ((ROT & 1) ? ((idx >> 2) + 2 * (idx & 3)) % 10 : 0)
#define P4_ROT ((ROT & 4) ? ((idx >> 2) + 2 * (idx & 3)) % 10 : 0)
__device__ __forceinline__ unsigned cvtpk(float lo, float hi) { return __builtin_bit_cast(unsigned, __builtin_convertvector(f32x2{lo, hi}, bf16x2)); }
__device__ __forceinline__ float bflo(unsigned v) { return __uint_as_float(v << 16); }
__device__ __forceinline__ float bfhi(unsigned v) { return __uint_as_float(v & 0xffff0000u); }
__device__ __forceinline__ s16x4 trread(lptr p) { return __builtin_amdgcn_ds_read_tr16_b64_v4i16((LAS s16x4*)p); }
__device__ __forceinline__ bf16x8 cat8(s16x4 a, s16x4 b) { return __builtin_shufflevector(a, b, 0, 1, 2, 3, 4, 5, 6, 7); }
__device__ __forceinline__ bf16x8 lds16(lptr p) { return *(LAS bf16x8*)p; }
#define MFMA16(a, b, c) __builtin_amdgcn_mfma_f32_16x16x32_bf16(a, b, c, 0, 0, 0)
#define MFMA32(a, b, c) __builtin_amdgcn_mfma_f32_32x32x16_bf16(a, b, c, 0, 0, 0)
#define MFMA32K8(a, b, c) __builtin_amdgcn_mfma_f32_32x32x8bf16_1k(a, b, c, 0, 0, 0)
#define SB() __builtin_amdgcn_sched_barrier(0)
#define SGB(mask, n) __builtin_amdgcn_sched_group_barrier(mask, n, 0)
__device__ __forceinline__ unsigned rfl(unsigned v) { return __builtin_amdgcn_readfirstlane(v); }
template <int CTRL> __device__ __forceinline__ float dppf(float v) { return __uint_as_float(__builtin_amdgcn_update_dpp(0u, __float_as_uint(v), CTRL, 0xf, 0xf, true)); }
__device__ __forceinline__ float add16(float v) { const auto t = __builtin_amdgcn_permlane16_swap(__float_as_uint(v), __float_as_uint(v), false, false); return __uint_as_float(t[0]) + __uint_as_float(t[1]); }
__device__ __forceinline__ float add32(float v) { const auto t = __builtin_amdgcn_permlane32_swap(__float_as_uint(v), __float_as_uint(v), false, false); return __uint_as_float(t[0]) + __uint_as_float(t[1]); }
__device__ __forceinline__ float sum32(float v) { v += dppf<0xB1>(v); v += dppf<0x4E>(v); v += dppf<0x141>(v); v += dppf<0x140>(v); return add16(v); }
__device__ __forceinline__ float bfr(float v) { return bflo(cvtpk(v, v)); }
__device__ __forceinline__ u32x4 mkdesc(const void* p, unsigned bytes) { const u64 a = (u64)p; return u32x4{(unsigned)a, (unsigned)(a >> 32) & 0xffffu, bytes, 0x00020000u}; }
template <int N> __device__ __forceinline__ void waitv() { asm volatile("s_waitcnt vmcnt(%0)" :: "i"(N) : "memory"); }
template <int N> __device__ __forceinline__ void waitv_bar() { SB(); asm volatile("s_waitcnt vmcnt(%0) lgkmcnt(0)\n\ts_barrier" :: "i"(N) : "memory"); SB(); }
__device__ __forceinline__ void bar_l() { SB(); asm volatile("s_waitcnt lgkmcnt(0)\n\ts_barrier" ::: "memory"); SB(); }

#define DMA1(o, sc) "buffer_load_dwordx4 %0, %1, %2 offen offset:" #o sc " lds\n\t"
template <int N, bool SC1> __device__ __forceinline__ void dma_run(unsigned voff, u32x4 d, unsigned soff, unsigned lds) {
#define DMA_ASM(body) asm volatile("s_mov_b32 m0, %3\n\ts_nop 0\n\t" body :: "v"(voff), "s"(d), "s"(soff), "s"(lds) : "memory")
  if constexpr (SC1) {
    if constexpr (N == 1) DMA_ASM(DMA1(0, " sc1"));
    if constexpr (N == 2) DMA_ASM(DMA1(0, " sc1") DMA1(1024, " sc1"));
    if constexpr (N == 3) DMA_ASM(DMA1(0, " sc1") DMA1(1024, " sc1") DMA1(2048, " sc1"));
    if constexpr (N == 4) DMA_ASM(DMA1(0, " sc1") DMA1(1024, " sc1") DMA1(2048, " sc1") DMA1(3072, " sc1"));
  } else {
    if constexpr (N == 1) DMA_ASM(DMA1(0, ""));
    if constexpr (N == 2) DMA_ASM(DMA1(0, "") DMA1(1024, ""));
    if constexpr (N == 3) DMA_ASM(DMA1(0, "") DMA1(1024, "") DMA1(2048, ""));
    if constexpr (N == 4) DMA_ASM(DMA1(0, "") DMA1(1024, "") DMA1(2048, "") DMA1(3072, ""));
  }
#undef DMA_ASM
}
__device__ __forceinline__ void dma4v(const unsigned (&v)[4], u32x4 d, unsigned soff, unsigned lds) {
  asm volatile("s_mov_b32 m0, %6\n\ts_nop 0\n\t"
               "buffer_load_dwordx4 %0, %4, %5 offen lds\n\t"
               "buffer_load_dwordx4 %1, %4, %5 offen offset:1024 lds\n\t"
               "buffer_load_dwordx4 %2, %4, %5 offen offset:2048 lds\n\t"
               "buffer_load_dwordx4 %3, %4, %5 offen offset:3072 lds"
               :: "v"(v[0]), "v"(v[1]), "v"(v[2]), "v"(v[3]), "s"(d), "s"(soff), "s"(lds) : "memory");
}

typedef __amdgpu_buffer_rsrc_t rsrc_t;
__device__ __forceinline__ void st8(rsrc_t r, unsigned voff, unsigned soff, u32x2 v) { __builtin_amdgcn_raw_buffer_store_b64(v, r, voff, soff, 16); }
__device__ __forceinline__ void st16(rsrc_t r, unsigned voff, unsigned soff, u32x4 v) { __builtin_amdgcn_raw_buffer_store_b128(v, r, voff + soff, 0, 16); }
__device__ __forceinline__ void st4(rsrc_t r, unsigned voff, unsigned soff, unsigned v) { __builtin_amdgcn_raw_buffer_store_b32(v, r, voff, soff, 16); }
__device__ __forceinline__ u32x4 ld16(rsrc_t r, unsigned voff, unsigned soff) { return __builtin_amdgcn_raw_buffer_load_b128(r, voff, soff, 16); }
__device__ __forceinline__ u32x2 ld8(rsrc_t r, unsigned voff, unsigned soff) { return __builtin_amdgcn_raw_buffer_load_b64(r, voff, soff, 16); }
__device__ __forceinline__ unsigned ld4(rsrc_t r, unsigned voff, unsigned soff) { return __builtin_amdgcn_raw_buffer_load_b32(r, voff, soff, 16); }

__device__ __forceinline__ unsigned read_tag(unsigned epoch_v, float dep) { unsigned t; asm volatile("s_nop 0\n\tv_readfirstlane_b32 %0, %1\n\ts_nop 4" : "=s"(t) : "v"(epoch_v), "v"(dep)); return t + 1u; }
__device__ __forceinline__ u32x2 flagval(unsigned tag) { return u32x2{tag, tag * 0x9E3779B1u + 0x7F4A7C15u}; }
template <unsigned STR = 128u>
__device__ __forceinline__ void poll_flags(rsrc_t r, int fidx, unsigned tag) {
  const u32x2 want = flagval(tag);
  const unsigned off = (unsigned)(fidx < 0 ? 0 : fidx) * STR;
#pragma unroll 1
  for (int spin = 0; spin < (1 << 16); ++spin) {
    const u32x2 v = ld8(r, off, WS_FLAG);
    if (__all(fidx < 0 || (v.x == want.x && v.y == want.y))) break;
    __builtin_amdgcn_s_sleep(1);
  }
  __builtin_amdgcn_fence(__ATOMIC_ACQUIRE, "wavefront");
  asm volatile("" ::: "memory");
}
__device__ __forceinline__ bool arrive_last(lptr L, int cnt, unsigned n) {
  asm volatile("s_waitcnt vmcnt(0) lgkmcnt(0)" ::: "memory");
  unsigned old = 0;
  if ((threadIdx.x & 63) == 0) old = __hip_atomic_fetch_add((LAS unsigned*)(L + L_CNT + 4 * cnt), 1u, __ATOMIC_RELAXED, __HIP_MEMORY_SCOPE_WORKGROUP);
  old = rfl(old);
  asm volatile("" ::: "memory");
  return old == n - 1;
}
__device__ __forceinline__ void group_sync(lptr L, int cnt, unsigned n) {
  asm volatile("s_waitcnt lgkmcnt(0)" ::: "memory");
  if ((threadIdx.x & 63) == 0) __hip_atomic_fetch_add((LAS unsigned*)(L + L_CNT + 4 * cnt), 1u, __ATOMIC_RELAXED, __HIP_MEMORY_SCOPE_WORKGROUP);
#pragma unroll 1
  for (int spin = 0; spin < (1 << 22); ++spin) { if (rfl(*(volatile LAS unsigned*)(L + L_CNT + 4 * cnt)) >= n) break; }
  asm volatile("" ::: "memory");
}
__device__ __forceinline__ void raise_flag(rsrc_t r, unsigned fidx, unsigned tag) { if ((threadIdx.x & 63) == 0) st8(r, 0, WS_FLAG + fidx * 128u, flagval(tag)); }
__device__ __forceinline__ void lds_signal(lptr L, int cnt) { asm volatile("" ::: "memory"); if ((threadIdx.x & 63) == 0) *(volatile LAS unsigned*)(L + L_CNT + 4 * cnt) = 1u; }
__device__ __forceinline__ void lds_wait(lptr L, int cnt) {
#pragma unroll 1
  for (int spin = 0; spin < (1 << 22); ++spin) { if (rfl(*(volatile LAS unsigned*)(L + L_CNT + 4 * cnt))) break; __builtin_amdgcn_s_sleep(2); }
  asm volatile("" ::: "memory");
}

__device__ __forceinline__ u32x4 pair16(u32x2 a, u32x2 b) {
  const auto x = __builtin_amdgcn_permlane16_swap(a.x, b.x, false, false), y = __builtin_amdgcn_permlane16_swap(a.y, b.y, false, false);
  return u32x4{x[0], y[0], x[1], y[1]};
}
__device__ __forceinline__ void retile_read(lptr u, int lane, u32x4 (&f)[4]) {
  const lptr rp = u + (8 * (lane >> 4) + ((lane & 15) >> 2)) * 128 + (lane & 3) * 8;
#pragma unroll
  for (int n = 0; n < 4; ++n) f[n] = __builtin_bit_cast(u32x4, cat8(trread(rp + n * 32), trread(rp + n * 32 + 512)));
}

__host__ __device__ constexpr u64 above_mask(int e) {
  u64 m = 0;
  for (int l = 0; l < 64; ++l) if ((e & 3) + 8 * (e >> 2) + 4 * (l >> 5) > (l & 31)) m |= 1ull << l;
  return m;
}

__global__ __launch_bounds__(512) __attribute__((amdgpu_waves_per_eu(2, 2)))
void mega(const u16* __restrict__ x, const u16* __restrict__ wqkv, const u16* __restrict__ wo, const u16* __restrict__ wg, const u16* __restrict__ wu,
          const u16* __restrict__ wd, u16* __restrict__ out, char* __restrict__ ws) {
  extern __shared__ __attribute__((aligned(16))) char smem[];
  const lptr L = (lptr)smem;
  const unsigned lds0 = (unsigned)(uintptr_t)L;
  const int bid = blockIdx.x, xcd = bid & 7, idx = bid >> 3;
  const int tid = threadIdx.x, lane = tid & 63, wid = rfl(tid >> 6), w4 = wid & 3;
  const bool isC = wid < 4;
  const int r = lane & 15, g = lane >> 4;
  const unsigned vo = lane * 16;
  const rsrc_t rws = __builtin_amdgcn_make_buffer_rsrc(ws, 0, WS_END, 0x00020000);
  const u32x4 dws = mkdesc(ws, WS_END);
  STAMP(0);
  unsigned epoch_v;
  asm volatile("buffer_load_dword %0, off, %1, 0 offset:0 sc1" : "=v"(epoch_v) : "s"(mkdesc(ws + WS_FLAG + F_EPOCH * 128, 128)) : "memory");
  if (tid < 16) *(LAS unsigned*)(L + L_CNT + 4 * tid) = 0u;
  unsigned tag;

  const int tm = 4 * (xcd >> 1) + (idx & 3), tn = (8 * (xcd & 1) + (idx >> 2)) ^ (idx >= 30 ? 8 : 0);
  LAS f32x2* st_tok = (LAS f32x2*)(L + L_MISC);
  LAS float* st_col = (LAS float*)(L + L_MISC + 1024);
  const int wgid = tm * 16 + tn;
  auto row_stats = [&]() {
    const int row = 128 * tm + 8 * tn + 2 * w4 + (lane >> 5);
    u32x2 xv[5];
#pragma unroll
    for (int i = 0; i < 5; ++i) xv[i] = *(const u32x2*)(x + row * D_ + ((lane & 31) + 32 * i) * 4);
    float s1 = 0.f, s2 = 0.f;
#pragma unroll
    for (int i = 0; i < 5; ++i) s1 += (bflo(xv[i].x) + bfhi(xv[i].x)) + (bflo(xv[i].y) + bfhi(xv[i].y));
    const float mean = sum32(s1) * (1.0f / D_);
#pragma unroll
    for (int i = 0; i < 5; ++i) { const float a = bflo(xv[i].x) - mean, b = bfhi(xv[i].x) - mean, c = bflo(xv[i].y) - mean, d = bfhi(xv[i].y) - mean; s2 += (a * a + b * b) + (c * c + d * d); }
    const float sd = bfr(__builtin_sqrtf(bfr(bfr(sum32(s2) * (1.0f / D_)) + LN_EPS)));
    const unsigned t = read_tag(epoch_v, sd), gt = flagval(t).y;
    if ((lane & 31) == 0) st16(rws, row * 16, WS_S1, u32x4{__float_as_uint(bfr(mean)), gt, __float_as_uint(1.0f / sd), gt});
    return t;
  };
  u32x4 rtf[2][4];
  unsigned rtd[2][4];
#pragma unroll
  for (int i = 0; i < 4; ++i) { rtf[0][i] = u32x4{0u, 0u, 0u, 0u}; rtf[1][i] = rtf[0][i]; rtd[0][i] = 0u; rtd[1][i] = 0u; }
  unsigned csd = 0u;
  if (tn < 15) {
    const int aid = tm * 15 + tn;
    const bool gu_ok = aid < 216, d_ok = aid < 180;
    const int cb = (gu_ok ? aid : 0) >> 2, isup = cb >= 27, cbb = cb - 27 * isup;
    const int sA = 5 * (aid & 3) + w4, sB = 5 * (aid & 3) + 4;
    const int ud = (d_ok ? aid : 0) * 3 + (w4 > 0 ? w4 - 1 : 0), dcb = ud / 54, dsx = ud - 54 * dcb;
    if (!isC) {
      __builtin_amdgcn_s_setprio(LPRIO);
      unsigned voffX[4], voffW[4];
#pragma unroll
      for (int i = 0; i < 4; ++i) {
        const int p = w4 * 4 + i;
        voffX[i] = (unsigned)((8 * p + (lane >> 3)) * 1280 + (((lane & 7) ^ ((lane >> 4) | (4 * (p & 1)))) * 16) - 1024 * i);
        voffW[i] = (unsigned)((4 * p + (lane >> 4)) * 3840 + ((((((lane & 15) >> 2) ^ (lane >> 4)) << 2) | (lane & 3)) * 16) - 1024 * i);
      }
      const u32x4 dx = mkdesc(x, 2048 * 1280), dw = mkdesc(wqkv, 640 * 3840);
      const unsigned xs0 = tm * 128 * 1280, ws0 = tn * 256, ld0 = lds0 + w4 * 4096;
      const int ks0 = tm < 10 ? tm : tm - 10;
      int ks = ks0, slot = 0;
#define P1_ISSUE() do { dma4v(voffX, dx, xs0 + ks * 128, ld0 + slot * P1_STAGE); dma4v(voffW, dw, ws0 + ks * (64 * 3840), ld0 + slot * P1_STAGE + 16384); \
        ks = ks == 9 ? 0 : ks + 1; slot = slot == 2 ? 0 : slot + 1; } while (0)
      const int tl = lane < 12 ? lane : 0;
      const char* tp = tl < 4 ? (const char*)x + (128 * tm + 16 * (idx >> 2) + 4 * w4 + tl) * 1280 : (const char*)wqkv + (16 * (idx & 3) + 4 * w4 + ((tl - 4) >> 1)) * 3840 + tn * 256 + (tl & 1) * 128;
      const int tstep = tl < 4 ? 128 : 64 * 3840;
      unsigned junk = 0;
#define T_ISSUE(st) do { if (TOUCH) asm volatile("global_load_dword %0, %1, off" : "+v"(junk) : "v"(tp + ((st) + ks0 >= 10 ? (st) + ks0 - 10 : (st) + ks0) * tstep) : "memory"); } while (0)
      constexpr int T1 = TOUCH ? 1 : 0;
      P1_ISSUE(); T_ISSUE(2);
      P1_ISSUE(); T_ISSUE(3); waitv_bar<8 + 2 * T1>();
      tag = read_tag(epoch_v, 0.f);
      const unsigned gt = flagval(tag).y;
      P1_ISSUE(); T_ISSUE(4);
      if (tm < 10) {
        const bf16x8 ones = {0x3F80, 0x3F80, 0x3F80, 0x3F80, 0x3F80, 0x3F80, 0x3F80, 0x3F80};
        const lptr cp = L + 16384 + (8 * g + (r >> 2)) * 256 + ((w4 ^ (r >> 2)) * 64) + (lane & 3) * 8;
        f32x4 c[2];
#pragma unroll
        for (int hf = 0; hf < 2; ++hf) {
          c[hf] = MFMA16(cat8(trread(cp + hf * 32), trread(cp + hf * 32 + 1024)), ones, (f32x4{0.f, 0.f, 0.f, 0.f}));
          c[hf] = MFMA16(cat8(trread(cp + hf * 32 + 8192), trread(cp + hf * 32 + 9216)), ones, c[hf]);
        }
        const f32x4 cc = (r & 2) ? c[1] : c[0];
        if (r < 4) st16(rws, (r >> 1) * 128 + g * 32 + (r & 1) * 16, WS_C1 + (tn * 10 + tm) * 1024 + w4 * 256,
                        u32x4{__float_as_uint((r & 1) ? cc[2] : cc[0]), gt, __float_as_uint((r & 1) ? cc[3] : cc[1]), gt});
      }
      waitv_bar<8 + 2 * T1>();
#pragma unroll 1
      for (int st = 3; st < 8; ++st) { P1_ISSUE(); T_ISSUE(st + 2); waitv_bar<8 + 2 * T1>(); }
      u32x4 gs, gc0, gc1, gc2;
      const unsigned gsv = (128 * tm + 32 * w4 + (lane & 31)) * 16 + WS_S1, gcv = WS_C1 + tn * 10240 + g * 1024 + w4 * 256 + r * 16, gcw = gcv + (g < 2 ? 8192 : 0);
      P1_ISSUE();
      asm volatile("buffer_load_dwordx4 %0, %4, %7, 0 offen sc1\n\tbuffer_load_dwordx4 %1, %5, %7, 0 offen sc1\n\tbuffer_load_dwordx4 %2, %5, %7, %8 offen sc1\n\tbuffer_load_dwordx4 %3, %6, %7, 0 offen sc1"
                   : "=&v"(gs), "=&v"(gc0), "=&v"(gc1), "=&v"(gc2) : "v"(gsv), "v"(gcv), "v"(gcw), "s"(dws), "s"(4096u) : "memory");
      waitv_bar<12 + T1>();
      P1_ISSUE();
      waitv_bar<12>();
      asm volatile("s_waitcnt vmcnt(0) lgkmcnt(0)\n\ts_barrier" : "+v"(gs), "+v"(gc0), "+v"(gc1), "+v"(gc2), "+v"(junk) :: "memory");
#pragma unroll 1
      for (int spin = 0; spin < (1 << 16); ++spin) {
        if (__all(gs.y == gt && gs.w == gt && gc0.y == gt && gc0.w == gt && gc1.y == gt && gc1.w == gt && gc2.y == gt && gc2.w == gt)) break;
        gs = ld16(rws, gsv, 0); gc0 = ld16(rws, gcv, 0); gc1 = ld16(rws, gcv, 4096); gc2 = ld16(rws, gcw, 0);
      }
      if (lane < 32) st_tok[32 * w4 + lane] = f32x2{__uint_as_float(gs.x), __uint_as_float(gs.z)};
      { const float ca = add32(add16(__uint_as_float(gc0.x) + __uint_as_float(gc1.x) + (g < 2 ? __uint_as_float(gc2.x) : 0.f)));
        const float cb = add32(add16(__uint_as_float(gc0.z) + __uint_as_float(gc1.z) + (g < 2 ? __uint_as_float(gc2.z) : 0.f)));
        if (lane < 16) *(LAS f32x2*)(st_col + w4 * 32 + lane * 2) = f32x2{ca, cb}; }
      bar_l();
      lds_wait(L, 12);
      const u32x4 dgu = mkdesc(isup ? wu : wg, 640 * 3456), dB = w4 == 0 ? dgu : mkdesc(wd, 1728 * 1280);
      const unsigned ldB = w4 == 0 ? 3456 : 1280;
      unsigned vA[4], vB[4];
#pragma unroll
      for (int i = 0; i < 4; ++i) { vA[i] = (8 * i + (lane >> 3)) * 3456 + (lane & 7) * 16 - 1024 * i; vB[i] = (8 * i + (lane >> 3)) * ldB + (lane & 7) * 16 - 1024 * i; }
      dma4v(vA, dgu, sA * (32 * 3456) + cbb * 128, lds0 + L_RT + w4 * 4096);
      dma4v(vB, dB, w4 == 0 ? sB * (32 * 3456) + cbb * 128 : dsx * (32 * 1280) + dcb * 128, lds0 + L_RT + 16384 + w4 * 4096);
#pragma unroll
      for (int n = 0; n < 4; ++n) {
        const int t = 4 * dcb + n, sl = t / 5;
        rtd[0][n] = gu_ok ? WS_WGU + ((sA * 108 + 4 * cbb + n) * 2 + isup) * 1024 : 0u;
        rtd[1][n] = w4 == 0 ? (gu_ok ? WS_WGU + ((sB * 108 + 4 * cbb + n) * 2 + isup) * 1024 : 0u) : (d_ok ? WS_WD + ((sl * 54 + dsx) * 5 + (t - 5 * sl)) * 1024 : 0u);
      }
      if (w4 == 1 && gu_ok) csd = WS_CS + ((aid & 3) * 3456 + cb * 64) * 4;
      waitv<0>();
      retile_read(L + L_RT + w4 * 4096, lane, rtf[0]);
      retile_read(L + L_RT + 16384 + w4 * 4096, lane, rtf[1]);
      { const bf16x8 ones = {0x3F80, 0x3F80, 0x3F80, 0x3F80, 0x3F80, 0x3F80, 0x3F80, 0x3F80};
#pragma unroll
        for (int n = 0; n < 4; ++n) {
          f32x4 c = MFMA16(__builtin_bit_cast(bf16x8, rtf[0][n]), ones, (f32x4{0.f, 0.f, 0.f, 0.f}));
          if (w4 == 0) c = MFMA16(__builtin_bit_cast(bf16x8, rtf[1][n]), ones, c);
          if (r == 0) *(LAS f32x4*)(L + L_GUCS + w4 * 256 + n * 64 + g * 16) = c;
        } }
    } else {
      const int wr = w4 >> 1, wc = w4 & 1;
      tag = row_stats();
      const int t32 = lane & 31, hh = lane >> 5;
      const int xo = (64 * wr + t32) * 128 + ((hh ^ ((t32 >> 1) & 7)) * 16);
      int wo_[2];
#pragma unroll
      for (int n = 0; n < 2; ++n) wo_[n] = 16384 + (8 * hh + (r >> 2)) * 256 + (((2 * wc + n) ^ (r >> 2)) * 64) + (g & 1) * 32 + (lane & 3) * 8;
      f32x16 acc[2][2];
#pragma unroll
      for (int n = 0; n < 2; ++n)
#pragma unroll
        for (int m = 0; m < 2; ++m)
#pragma unroll
          for (int e = 0; e < 16; ++e) acc[n][m][e] = 0.f;
      bf16x8 xa[2][2], wa[2][2], xb[2][2], wb[2][2];
#define P1_READ(xf, wf, B, kk) do { _Pragma("unroll") for (int k = 0; k < 2; ++k) { \
        xf[k][0] = lds16(B + (xo ^ (64 * kk + 32 * k))); \
        wf[k][0] = cat8(trread(B + wo_[0] + 8192 * kk + 4096 * k), trread(B + wo_[0] + 8192 * kk + 4096 * k + 1024)); \
        xf[k][1] = lds16(B + (xo ^ (64 * kk + 32 * k)) + 4096); \
        wf[k][1] = cat8(trread(B + wo_[1] + 8192 * kk + 4096 * k), trread(B + wo_[1] + 8192 * kk + 4096 * k + 1024)); } } while (0)
#define P1_MMA(xf, wf) do { _Pragma("unroll") for (int k = 0; k < 2; ++k) _Pragma("unroll") for (int n = 0; n < 2; ++n) _Pragma("unroll") for (int m = 0; m < 2; ++m) \
        acc[n][m] = MFMA32(wf[k][n], xf[k][m], acc[n][m]); \
        _Pragma("unroll") for (int i = 0; i < 6; ++i) { SGB(0x008, 1); SGB(0x100, 2); } SGB(0x008, 2); } while (0)
      unsigned junk = 0;
      { const char* gp = (const char*)(isup ? wu : wg) + cbb * 128;
        const char* tp = (lane < 32 || w4 == 0) ? gp + ((lane < 32 ? sA : sB) * 32 + (lane & 31)) * 3456 : (const char*)wd + (dsx * 32 + (lane & 31)) * 1280 + dcb * 128;
        bar_l();
        if (RTOUCH) asm volatile("global_load_dword %0, %1, off" : "+v"(junk) : "v"(tp) : "memory"); }
      STAMP(17);
      P1_READ(xa, wa, L, 0);
      SB();
      int slot = 0;
#pragma unroll 1
      for (int st = 0; st < 10; ++st) {
        const lptr B = L + slot * P1_STAGE;
        slot = slot == 2 ? 0 : slot + 1;
        const lptr Bn = L + slot * P1_STAGE;
        P1_READ(xb, wb, B, 1);
        P1_MMA(xa, wa);
        bar_l();
        P1_READ(xa, wa, Bn, 0);
        P1_MMA(xb, wb);
        SB();
      }
      STAMP(1);
      const int tensor = tn / 5;
      const float osc = tensor == 0 ? QSCALE : 1.0f;
      f32x2 ts[2];
#pragma unroll
      for (int m = 0; m < 2; ++m) { ts[m] = st_tok[64 * wr + 32 * m + t32]; ts[m].y *= osc; }
      const unsigned so = WS_QKV + (tensor * 32 * 64 + 4 * tm + 2 * wr) * 1280, dv0 = (g & 1) * 1280 + (r >> 2) * 320 + (r & 3) * 16;
#pragma unroll
      for (int n = 0; n < 2; ++n)
#pragma unroll
        for (int i = 0; i < 4; ++i) {
          const f32x4 cs = *(LAS f32x4*)(st_col + 64 * wc + 32 * n + 8 * i + 4 * hh);
          const int c4 = (tn - 5 * tensor) * 32 + 16 * wc + 8 * n + 2 * i + hh;
          const int head = (c4 * 205) >> 10, dg = c4 - 5 * head;
          const f32x4 v = (f32x4{acc[n][0][4 * i], acc[n][0][4 * i + 1], acc[n][0][4 * i + 2], acc[n][0][4 * i + 3]} - ts[0].x * cs) * ts[0].y;
          const f32x4 w = (f32x4{acc[n][1][4 * i], acc[n][1][4 * i + 1], acc[n][1][4 * i + 2], acc[n][1][4 * i + 3]} - ts[1].x * cs) * ts[1].y;
          st16(rws, dv0 + head * (64 * 1280) + dg * 64, so, pair16(u32x2{cvtpk(v[0], v[1]), cvtpk(v[2], v[3])}, u32x2{cvtpk(w[0], w[1]), cvtpk(w[2], w[3])}));
        }
      STAMP(2);
      if (w4 == 3) lds_signal(L, 12);
      asm volatile("s_waitcnt vmcnt(0)" : "+v"(junk) :: "memory");
      if (arrive_last(L, 0, 4)) raise_flag(rws, F_Q + wgid, tag);
    }
  } else {
    if (isC) (void)row_stats();
    else if (WARM) {
      unsigned acc = 0;
      if (w4 == 0) {
        const char* pc; int len;
        asm volatile("s_getpc_b64 %0\n.Lmega_here:\n\ts_mov_b32 %1, .Lmega_tail-.Lmega_here" : "=s"(pc), "=s"(len));
#pragma unroll
        for (int i = 0; i < 5; ++i) { const int o = (i * 64 + lane) * 128; acc += *(const volatile unsigned*)(pc + (o < len ? o : 0)); }
      } else if (w4 == 1) {
        const int o = (lane < 34 ? lane : 33) * 32768;
        acc += *(const volatile u16*)(wg + o) + *(const volatile u16*)(wu + o) + *(const volatile u16*)(wd + o) + *(const volatile u16*)(out + (lane < 40 ? lane : 39) * 32768);
      } else {
#pragma unroll
        for (int i = 0; i < 4; ++i) acc += ld4(rws, ((w4 - 2) * 256 + i * 64 + lane) * 65536u, 0);
      }
      asm volatile("" :: "v"(acc));
    }
    const u32x4 dwo = mkdesc(wo, 640 * 1280);
    const int u0 = tm * 8 + wid;
    unsigned wod[2][4];
#pragma unroll
    for (int k = 0; k < 2; ++k) {
      const int u = (k == 1 && u0 + 128 < 200) ? u0 + 128 : u0, s0 = u / 10, c0 = u - 10 * s0, j = lane >> 3;
      unsigned vd[4];
#pragma unroll
      for (int i = 0; i < 4; ++i) {
        const int ch = 4 * s0 + i;
        const int row = ch < 64 ? 20 * (ch >> 1) + 4 * (ch & 1) + (j & 3) + 8 * (j >> 2) : 20 * (2 * (ch - 64) + (j >> 2)) + 16 + (j & 3);
        vd[i] = row * 1280 + (lane & 7) * 16 - 1024 * i;
        const int t = 4 * c0 + i, sl = t / 5;
        wod[k][i] = WS_WO + ((sl * 20 + s0) * 5 + (t - 5 * sl)) * 1024;
      }
      dma4v(vd, dwo, c0 * 128, lds0 + L_RT - k * 32768 + wid * 4096);
    }
    waitv_bar<0>();
    tag = read_tag(epoch_v, 0.f);
#pragma unroll
    for (int k = 0; k < 2; ++k) {
      u32x4 f[4];
      retile_read(L + L_RT - k * 32768 + wid * 4096, lane, f);
#pragma unroll
      for (int n = 0; n < 4; ++n) st16(rws, vo, wod[k][n], f[n]);
    }
    if (arrive_last(L, 5, 8)) raise_flag(rws, F_Q + wgid, tag);
  }
  STAMP(3);

  const int head = 4 * xcd + (idx >> 3), qb = idx & 7;
  const int rt = 4 * xcd + (idx & 3), ct = idx >> 2;
  if (isC) {
    if (wid == 0) {
      int f = -1;
      if (lane < 24) {
        const int tmi = lane / 6, wh = lane - 6 * tmi, tmm = 2 * qb - 2 + tmi, ten = wh >> 1;
        const int tnn = 5 * ten + ((20 * head + ((wh & 1) ? 19 : 0)) >> 7);
        if (tmm >= 0 && (ten > 0 || tmi >= 2)) f = F_Q + tmm * 16 + tnn;
      } else if (lane < 40) f = F_Q + (lane - 24) * 16 + 15;
      poll_flags(rws, f, tag);
      lds_signal(L, 8);
    } else lds_wait(L, 8);
    STAMP(4);
    const unsigned hb = WS_QKV + (head * 64 + 8 * qb) * 1280;
    const unsigned kv = hb + (32 * 64 * 1280) + (w4 & 1) * 5120 - ((w4 < 2 && qb > 0) ? 10240 : 0);
    if (w4 < 2) dma_run<3, true>(vo, dws, hb + w4 * 3072, lds0 + A_Q + w4 * 3072);
    else dma_run<2, true>(vo, dws, hb + 6144 + (w4 - 2) * 2048, lds0 + A_Q + 6144 + (w4 - 2) * 2048);
    dma_run<4, true>(vo, dws, kv, lds0 + A_K + w4 * 5120); dma_run<1, true>(vo, dws, kv + 4096, lds0 + A_K + w4 * 5120 + 4096);
    dma_run<4, true>(vo, dws, kv + 32 * 64 * 1280, lds0 + A_V + w4 * 5120); dma_run<1, true>(vo, dws, kv + 32 * 64 * 1280 + 4096, lds0 + A_V + w4 * 5120 + 4096);
  } else if (wid == 4) *(LAS u32x4*)(L + A_ONE + vo) = u32x4{0x00003F80u, 0u, 0x00003F80u, 0u};
  __builtin_amdgcn_s_setprio(0);
  u32x4 ma, mb;
#pragma unroll
  for (int e = 0; e < 4; ++e) {
    const int ka = 2 * (e & 1) + 8 * (e >> 1) + 4 * (lane >> 5), kb = ka + 16, c = lane & 31;
    ma[e] = (ka > c ? 0xffffu : 0u) | (ka + 1 > c ? 0xffff0000u : 0u);
    mb[e] = (kb > c ? 0xffffu : 0u) | (kb + 1 > c ? 0xffff0000u : 0u);
  }
  asm volatile("" : "+v"(ma), "+v"(mb));
  waitv_bar<5>();
  STAMP(5);
  if (!isC) {
#pragma unroll
    for (int i = 0; i < 8; ++i) if (rtd[i >> 2][i & 3]) st16(rws, vo, rtd[i >> 2][i & 3], rtf[i >> 2][i & 3]);
  }
  if (csd) { const LAS float* cp = (const LAS float*)(L + L_GUCS) + lane; st4(rws, lane * 4, csd, __float_as_uint((cp[0] + cp[64]) + (cp[128] + cp[192]))); }
  {
    const int c = lane & 31, hh = lane >> 5;
    const int nskip = qb == 0 ? 8 - wid : 0;
    const unsigned wos = WS_WO + ct * 102400 + wid * 12288, wod = lds0 + L_WO + wid * 12288;
#define WO_PIECE(i) dma_run<1, true>(vo, dws, wos + (i) * 1024, wod + (i) * 1024)
    bf16x8 q1; u32x2 q2;
    const int ko = wid * 1280 + ((c >> 2) & 3) * 320 + (c & 3) * 16 + (c >> 4) * 8;
#define LD8(p) (*(volatile LAS s16x4*)(p))
    { const lptr qp = L + A_Q + ko;
      q1 = cat8(LD8(qp + 128 * hh), LD8(qp + 128 * hh + 64));
      q2 = __builtin_bit_cast(u32x2, LD8(qp + 256)); if (hh) q2 = u32x2{0x3F80u, 0u}; }
    const lptr kp = L + A_K + ko;
    f32x16 s[9];
    const f32x16 zero16 = {0.f,0.f,0.f,0.f,0.f,0.f,0.f,0.f,0.f,0.f,0.f,0.f,0.f,0.f,0.f,0.f};
    bf16x8 k1[9]; u32x2 k2[9];
#pragma unroll
    for (int jj = 0; jj < 9; ++jj) {
      const int jb = jj == 0 ? 0 : jj == 1 ? 8 : jj - 1;
      k1[jb] = cat8(LD8(kp + jb * 1280 + 128 * hh), LD8(kp + jb * 1280 + 128 * hh + 64));
      k2[jb] = __builtin_bit_cast(u32x2, LD8(kp + jb * 1280 + 256));
    }
    SB();
    float m0, m1;
#pragma unroll
    for (int jj = 0; jj < 11; ++jj) {
      if (jj < 9) {
        const int jb = jj == 0 ? 0 : jj == 1 ? 8 : jj - 1;
        k2[jb].x = hh ? (jb < nskip ? 0xFF7Fu : 0u) : k2[jb].x;
        s[jb] = MFMA32(k1[jb], q1, zero16);
        s[jb] = MFMA32K8(__builtin_bit_cast(s16x4, k2[jb]), __builtin_bit_cast(s16x4, q2), s[jb]);
      }
      if (jj == 3) {
#pragma unroll
        for (int e = 0; e < 16; ++e) s[0][e] = __builtin_amdgcn_inverse_ballot_w64(above_mask(e)) ? s[0][e] : s[8][e];
        m0 = fmaxf(s[0][0], s[0][1]); m1 = fmaxf(s[0][2], s[0][3]);
#pragma unroll
        for (int e = 4; e < 16; e += 4) { m0 = fmaxf(fmaxf(m0, s[0][e]), s[0][e + 1]); m1 = fmaxf(fmaxf(m1, s[0][e + 2]), s[0][e + 3]); }
      }
      if (jj >= 4) {
        const int jb = jj - 3;
#pragma unroll
        for (int e = 0; e < 16; e += 4) { m0 = fmaxf(fmaxf(m0, s[jb][e]), s[jb][e + 1]); m1 = fmaxf(fmaxf(m1, s[jb][e + 2]), s[jb][e + 3]); }
      }
      if (jj & 1) WO_PIECE(jj >> 1);
      SB();
    }
    float mx = fmaxf(m0, m1);
    { auto rr = __builtin_amdgcn_permlane32_swap(__float_as_uint(mx), __float_as_uint(mx), false, false);
      mx = fmaxf(__uint_as_float(rr[0]), __uint_as_float(rr[1])); }
    STAMP(28);
    if (isC) { waitv<5>(); if (lane == 0) __hip_atomic_fetch_add((LAS unsigned*)(L + L_CNT + 4 * 7), 1u, __ATOMIC_RELAXED, __HIP_MEMORY_SCOPE_WORKGROUP); }
    u32x2 nm;
    { const unsigned hi = __float_as_uint(-mx) & 0xffff0000u; const float r1 = -mx - __uint_as_float(hi);
      const unsigned mid = __float_as_uint(r1) & 0xffff0000u, lo = __float_as_uint(r1 - __uint_as_float(mid));
      nm = hh ? u32x2{0u, 0u} : u32x2{(hi >> 16) | mid, lo >> 16}; }
    const s16x4 one4 = {0x3F80, 0x3F80, 0x3F80, 0x3F80};
#define S_SHIFT(jb) s[jb] = MFMA32K8(one4, __builtin_bit_cast(s16x4, nm), s[jb])
    S_SHIFT(1);
#pragma unroll 1
    for (int spin = 0; spin < (1 << 22); ++spin) { if (rfl(*(volatile LAS unsigned*)(L + L_CNT + 4 * 7)) >= 4u) break; }
    asm volatile("" ::: "memory");
    SB();
    STAMP(29);
    const int gq = r >> 2, gp = lane & 3, up16 = (lane >> 4) & 1;
    const bool vconst = up16 && gp > 0;
    const lptr vb0 = L + (vconst ? A_ONE + 8 + gq * 16 : A_V + wid * 1280 + hh * 320 + (4 * up16 + gp) * 64 + gq * 16);
    const int vstep = vconst ? 0 : 1280;
    f32x16 o = zero16;
    bf16x8 va[2], vb[2], vc, vd;
#define V_READ(a, b, jb) do { const lptr v0 = vb0 + (jb) * vstep; a = cat8(trread(v0), trread(v0 + 640)); b = cat8(trread(v0 + 8), trread(v0 + 648)); } while (0)
    u32x4 pa[2], pb[2];
#pragma unroll
    for (int it = 0; it < 9; ++it) {
      const int jb = it == 7 ? 0 : it + 1;
      if (it < 8) {
        V_READ(va[it & 1], vb[it & 1], jb);
        if (it == 7) V_READ(vc, vd, 8);
        if (it < 7) S_SHIFT(it == 6 ? 0 : it + 2);
        float p[16];
#pragma unroll
        for (int e = 0; e < 16; ++e) p[e] = __builtin_amdgcn_exp2f(s[jb][e]);
        pa[it & 1] = u32x4{cvtpk(p[0], p[1]), cvtpk(p[2], p[3]), cvtpk(p[4], p[5]), cvtpk(p[6], p[7])};
        pb[it & 1] = u32x4{cvtpk(p[8], p[9]), cvtpk(p[10], p[11]), cvtpk(p[12], p[13]), cvtpk(p[14], p[15])};
      }
      if (it >= 1 && it < 8) {
        o = MFMA32(va[(it - 1) & 1], __builtin_bit_cast(bf16x8, pa[(it - 1) & 1]), o);
        o = MFMA32(vb[(it - 1) & 1], __builtin_bit_cast(bf16x8, pb[(it - 1) & 1]), o);
        SGB(0x100, 4); SGB(0x008, 1); SGB(0x402, 5); SGB(0x008, 1); SGB(0x402, 5); SGB(0x008, 1);
      }
      if (it == 8) {
        o = MFMA32(va[1], __builtin_bit_cast(bf16x8, pa[1] & ma), o);
        o = MFMA32(vb[1], __builtin_bit_cast(bf16x8, pb[1] & mb), o);
        o = MFMA32(vc, __builtin_bit_cast(bf16x8, pa[1] & ~ma), o);
        o = MFMA32(vd, __builtin_bit_cast(bf16x8, pb[1] & ~mb), o);
      }
      if (it < 7) WO_PIECE(5 + it);
      if (it == 7) dma_run<1, true>(vo, dws, WS_WO + ct * 102400 + 98304 + w4 * 1024, lds0 + L_WO + 98304 + w4 * 1024);
      SB();
    }
    const float rl = __builtin_amdgcn_rcpf(hh ? o[8] : o[12]);
    const unsigned ob = WS_O + (16 * qb + 2 * wid) * 20480, ov = (c >> 4) * 20480 + (c & 15) * 16;
    st16(rws, ov + hh * 256, ob + head * 512, u32x4{cvtpk(o[0] * rl, o[1] * rl), cvtpk(o[2] * rl, o[3] * rl), cvtpk(o[4] * rl, o[5] * rl), cvtpk(o[6] * rl, o[7] * rl)});
    if (hh == 0) st8(rws, ov, ob + 16384 + (head >> 1) * 256 + (head & 1) * 8, u32x2{cvtpk(o[8] * rl, o[9] * rl), cvtpk(o[10] * rl, o[11] * rl)});
    STAMP(6);
  }
  waitv_bar<0>();
  if (wid == 0) raise_flag(rws, F_O + qb * 32 + head, tag);
  if (wid == 1) raise_flag(rws, F_RT + (tn < 15 ? tm * 15 + tn : 240 + tm), tag);
  STAMP(7);

  const int pn = 4 * (xcd >> 1) + (idx & 3), cgp = 8 * (xcd & 1) + (idx >> 2);
  const int t0 = cgp < 12 ? 7 * cgp : 6 * cgp + 12, ntile = cgp < 12 ? 7 : 6;
  const int T5 = 4 * rt + w4;
  u32x2 hreg[5];
  const unsigned wgs = WS_WGU + t0 * 2048 + (w4 >> 1) * (108 * 2048) + (w4 & 1) * 7168;
  const int rot4 = P4_ROT;
#define KS4(st) ((st) + rot4 >= 10 ? (st) + rot4 - 10 : (st) + rot4)
  if (isC) {
    u32x2 xv[5];
#pragma unroll
    for (int n = 0; n < 5; ++n) xv[n] = *(const u32x2*)(x + (size_t)(16 * T5 + r) * D_ + 80 * ct + 16 * n + 4 * g);
    if (wid == 0) { poll_flags(rws, lane < 32 ? (int)(F_O + (rt >> 2) * 32 + lane) : -1, tag); lds_signal(L, 9); }
    else lds_wait(L, 9);
    STAMP(8);
    u32x4 af[20];
    const int rot3 = (ROT & 2) ? (ct * 5) >> 3 : 0;
#pragma unroll
    for (int s = 0; s < 20; ++s) { int sg = (s >> 2) + rot3; sg = sg >= 5 ? sg - 5 : sg; af[s] = ld16(rws, vo + (s & 3) * 1024, WS_O + T5 * 20480 + sg * 4096); }
    SB();
    f32x4 acc[5];
#pragma unroll
    for (int n = 0; n < 5; ++n) acc[n] = f32x4{0.f, 0.f, 0.f, 0.f};
    bf16x8 wf[2][5];
    lptr B = L + L_WO + vo + rot3 * 20480;
#pragma unroll
    for (int n = 0; n < 5; ++n) wf[0][n] = lds16(B + n * 1024);
    SB();
#pragma unroll
    for (int s = 0; s < 20; ++s) {
      if (s + 1 < 20) {
        if (((s + 1) & 3) == 0) { int sg = ((s + 1) >> 2) + rot3; sg = sg >= 5 ? sg - 5 : sg; B = L + L_WO + vo + sg * 20480; }
#pragma unroll
        for (int n = 0; n < 5; ++n) wf[(s + 1) & 1][n] = lds16(B + (((s + 1) & 3) * 5 + n) * 1024);
      }
#pragma unroll
      for (int n = 0; n < 5; ++n) acc[n] = MFMA16(wf[s & 1][n], __builtin_bit_cast(bf16x8, af[s]), acc[n]);
      SB();
    }
    bar_l();
    STAMP(9);
    float s1 = 0.f, s2 = 0.f;
#pragma unroll
    for (int n = 0; n < 5; ++n)
      hreg[n] = u32x2{cvtpk(acc[n][0] + bflo(xv[n].x), acc[n][1] + bfhi(xv[n].x)), cvtpk(acc[n][2] + bflo(xv[n].y), acc[n][3] + bfhi(xv[n].y))};
    { const unsigned hv = (g & 1) * 512 + (g >> 1) * 256 + r * 16, hsb = WS_H + T5 * 20480 + ct * 2560;
      st16(rws, hv, hsb, pair16(hreg[0], hreg[1]));
      st16(rws, hv, hsb + 1024, pair16(hreg[2], hreg[3]));
      const u32x4 last = pair16(hreg[4], hreg[4]);
      if (!(g & 1)) st16(rws, hv, hsb + 2048, last); }
#pragma unroll
    for (int n = 0; n < 5; ++n) {
      const float a = bflo(hreg[n].x), b = bfhi(hreg[n].x), c = bflo(hreg[n].y), d = bfhi(hreg[n].y);
      s1 += (a + b) + (c + d); s2 += (a * a + b * b) + (c * c + d * d);
    }
    s1 = add32(add16(s1)); s2 = add32(add16(s2));
    if (g == 0) st8(rws, r * 8, WS_HST + (T5 * 8 + ct) * 128, u32x2{__float_as_uint(s1), __float_as_uint(s2)});
    if (arrive_last(L, 3, 4)) raise_flag(rws, F_H + rt * 8 + ct, tag);
  } else {
    __builtin_amdgcn_s_setprio(LPRIO);
    int f = -1;
    if (lane < 24) { const int b = lane >> 2, isup = b >= 3, cb = (t0 >> 2) + b - 3 * isup; if (cb <= ((t0 + ntile - 1) >> 2)) f = F_RT + (cb + 27 * isup) * 4 + (lane & 3); }
    else if (lane < 60) { const int b = (lane - 24) / 18; f = F_RT + (((5 * ct) >> 2) + b) * 18 + (lane - 24 - 18 * b); }
    if (w4 == 0) { poll_flags(rws, f, tag); lds_signal(L, 6); } else lds_wait(L, 6);
#pragma unroll
    for (int st = 0; st < 2; ++st) {
      dma_run<4, true>(vo, dws, wgs + KS4(st) * (2 * 108 * 2048), lds0 + st * P4_W + w4 * 7168);
      dma_run<3, true>(vo, dws, wgs + KS4(st) * (2 * 108 * 2048) + 4096, lds0 + st * P4_W + w4 * 7168 + 4096);
    }
    const int cc = w4 * 64 + lane;
    if (cc < 224) {
      const unsigned o = (cc >= 112 ? 1728 - 112 : 0) + cc + t0 * 16;
      const float t = (__uint_as_float(ld4(rws, o * 4, WS_CS)) + __uint_as_float(ld4(rws, o * 4, WS_CS + 3456 * 4))) +
                      (__uint_as_float(ld4(rws, o * 4, WS_CS + 2 * 3456 * 4)) + __uint_as_float(ld4(rws, o * 4, WS_CS + 3 * 3456 * 4)));
      *(LAS float*)(L + L_MISC + cc * 4) = t;
    }
#pragma unroll
    for (int n = 0; n < 5; ++n) hreg[n] = u32x2{0u, 0u};
    bar_l();
  }
  STAMP(10);

  const int T4 = 8 * pn + 2 * w4;
  const int rot5 = (ROT & 8) ? ct : 0;
#define KS5(st) ((st) + rot5 >= 9 ? (st) + rot5 - 9 : (st) + rot5)
  if (!isC) {
    poll_flags(rws, lane < 8 ? (int)(F_H + (2 * pn + (w4 >> 1)) * 8 + lane) : -1, tag);
    STAMP(11);
    const unsigned as = WS_H + T4 * 20480, ad = lds0 + P4_A0 + w4 * 4096;
#pragma unroll
    for (int st = 0; st < 2; ++st) { dma_run<2, true>(vo, dws, as + KS4(st) * 2048, ad + st * P4_A); dma_run<2, true>(vo, dws, as + 20480 + KS4(st) * 2048, ad + st * P4_A + 2048); }
    u32x2 hs[2][2];
#pragma unroll
    for (int m = 0; m < 2; ++m) { hs[m][0] = ld8(rws, lane * 8, WS_HST + (T4 + m) * 1024); hs[m][1] = ld8(rws, lane * 8, WS_HST + (T4 + m) * 1024 + 512); }
    waitv_bar<8>();
    int slot = 2;
#pragma unroll 1
    for (int st = 2; st < 10; ++st) {
      const int ks = KS4(st);
      dma_run<2, true>(vo, dws, as + ks * 2048, ad + slot * P4_A); dma_run<2, true>(vo, dws, as + 20480 + ks * 2048, ad + slot * P4_A + 2048);
      dma_run<4, true>(vo, dws, wgs + ks * (2 * 108 * 2048), lds0 + slot * P4_W + w4 * 7168);
      dma_run<3, true>(vo, dws, wgs + ks * (2 * 108 * 2048) + 4096, lds0 + slot * P4_W + w4 * 7168 + 4096);
      slot = slot == 2 ? 0 : slot + 1;
      waitv_bar<11>();
    }
    waitv_bar<0>();
#pragma unroll
    for (int m = 0; m < 2; ++m) {
      const float s1 = add32(add16(__uint_as_float(hs[m][0].x) + __uint_as_float(hs[m][1].x))), s2 = add32(add16(__uint_as_float(hs[m][0].y) + __uint_as_float(hs[m][1].y)));
      const float mean = s1 * (1.0f / D_);
      if (g == 0) *(LAS f32x2*)(L + L_MISC + 896 + (w4 * 32 + m * 16 + r) * 8) = f32x2{mean, fmaxf(s2 * (1.0f / D_) - mean * mean, 0.f) + LN_EPS};
    }
    bar_l();
    STAMP(12);
    if (bid == 0) {
      poll_flags(rws, F_RT + w4 * 64 + lane, tag);
      group_sync(L, 11, 4);
      if (tid == 256) st4(rws, 0, WS_FLAG + F_EPOCH * 128, tag);
    }
    const unsigned wpo = w4 * 7168 + (w4 < 2 ? w4 : 2) * 1024, wds = WS_WD + ct * (54 * 5120) + wpo, wdd = lds0 + wpo;
#define P5_ISSUE(st, slot) do { const unsigned so_ = wds + KS5(st) * P5_STAGE, do_ = wdd + (slot) * P5_STAGE; \
      dma_run<4, true>(vo, dws, so_, do_); if (w4 < 2) dma_run<4, true>(vo, dws, so_ + 4096, do_ + 4096); else dma_run<3, true>(vo, dws, so_ + 4096, do_ + 4096); } while (0)
#pragma unroll 1
    for (int st = 0; st < 5; ++st) {
      if (st == P5_PRE) bar_l();
      P5_ISSUE(st, st);
    }
    if (P5_PRE >= 5) bar_l();
    waitv_bar<21>();
    waitv_bar<14>();
#pragma unroll 1
    for (int st = 5; st < 9; ++st) { P5_ISSUE(st, st - 5); waitv_bar<14>(); }
    waitv_bar<7>(); waitv_bar<0>();
  } else {
    f32x4 acc[7][2][2];
#pragma unroll
    for (int n = 0; n < 7; ++n)
#pragma unroll
      for (int u = 0; u < 2; ++u) { acc[n][u][0] = f32x4{0.f, 0.f, 0.f, 0.f}; acc[n][u][1] = acc[n][u][0]; }
    bf16x8 a0[2], a1[2], w0[7], w1[7];
#define P4_RW(wf, W, kk, half) do { _Pragma("unroll") for (int i = 0; i < 7; ++i) wf[i] = lds16(W + (kk) * 14336 + ((half) * 7 + i) * 1024); } while (0)
#define P4_RA(af, A, kk) do { af[0] = lds16(A + (kk) * 1024); af[1] = lds16(A + 2048 + (kk) * 1024); } while (0)
#define P4_MMA(wf, af, half, nrd) do { _Pragma("unroll") for (int i = 0; i < 7; ++i) { const int t = (half) * 7 + i; \
        acc[t >> 1][t & 1][0] = MFMA16(wf[i], af[0], acc[t >> 1][t & 1][0]); acc[t >> 1][t & 1][1] = MFMA16(wf[i], af[1], acc[t >> 1][t & 1][1]); } \
        _Pragma("unroll") for (int i = 0; i < nrd; ++i) { SGB(0x008, 1); SGB(0x100, 1); } SGB(0x008, 14 - nrd); SB(); } while (0)
    bar_l();
    STAMP(11);
    unsigned junk = 0;
    const unsigned dtv = ((idx & 3) * 60 + w4 * 15 + (lane < 15 ? lane : 14)) * 128;
    P4_RA(a0, L + P4_A0 + w4 * 4096 + vo, 0); P4_RW(w0, L + vo, 0, 0);
    SB();
    int slot = 0;
#pragma unroll 1
    for (int st = 0; st < 10; ++st) {
      const lptr W = L + slot * P4_W + vo, A = L + P4_A0 + slot * P4_A + w4 * 4096 + vo;
      slot = slot == 2 ? 0 : slot + 1;
      const lptr Wn = L + slot * P4_W + vo, An = L + P4_A0 + slot * P4_A + w4 * 4096 + vo;
      P4_RW(w1, W, 0, 1);                     P4_MMA(w0, a0, 0, 7);
      P4_RA(a1, A, 1); P4_RW(w0, W, 1, 0);    P4_MMA(w1, a0, 1, 9);
      P4_RW(w1, W, 1, 1);                     P4_MMA(w0, a1, 0, 7);
      if (DTOUCH && st < 9) asm volatile("buffer_load_dword %0, %1, %2, %3 offen sc1" : "+v"(junk) : "v"(dtv), "s"(dws), "s"(WS_WD + ct * (54 * 5120) + KS5(st) * P5_STAGE) : "memory");
      bar_l();
      P4_RA(a0, An, 0); P4_RW(w0, Wn, 0, 0);  P4_MMA(w1, a1, 1, 9);
    }
    STAMP(12);
    constexpr float C1 = -2.0f * 0.7978845608028654f * 1.4426950408889634f, C2 = C1 * 0.044715f;
    u32x2 gq[2][7];
#pragma unroll
    for (int m = 0; m < 2; ++m) {
      const f32x2 tk = *(LAS f32x2*)(L + L_MISC + 896 + (w4 * 32 + m * 16 + r) * 8);
      const float mean = tk.x, var = tk.y;
      const float sc = __builtin_amdgcn_rsqf(var), c1s = C1 * sc, c2s = C2 * sc * sc * sc;
#pragma unroll
      for (int n = 0; n < 7; ++n) {
        const f32x4 cg = *(LAS f32x4*)(L + L_MISC + (n * 16 + 4 * g) * 4), cu = *(LAS f32x4*)(L + L_MISC + (112 + n * 16 + 4 * g) * 4);
        const f32x4 a = acc[n][0][m] - mean * cg, b = acc[n][1][m] - mean * cu;
        const f32x4 z = a * (a * a * c2s + c1s);
        f32x4 ex;
#pragma unroll
        for (int e = 0; e < 4; ++e) ex[e] = __builtin_amdgcn_exp2f(z[e]);
        const f32x4 dn = ex * var + var;
        f32x4 rc;
#pragma unroll
        for (int e = 0; e < 4; ++e) rc[e] = __builtin_amdgcn_rcpf(dn[e]);
        const f32x4 o = (a * b) * rc;
        gq[m][n] = u32x2{cvtpk(o[0], o[1]), cvtpk(o[2], o[3])};
      }
    }
#pragma unroll
    for (int n = 0; n < 7; ++n) {
      const u32x4 v = pair16(gq[0][n], gq[1][n]);
      if (n < ntile) st16(rws, (g & 1) * (54 * 1024) + (g >> 1) * 256 + r * 16, WS_G + T4 * (54 * 1024) + (t0 + n) * 512, v);
    }
    STAMP(13);
    waitv_bar<0>();
    asm volatile("" :: "v"(junk));
    if (wid == 0) raise_flag(rws, F_G + pn * 16 + cgp, tag);

    if (wid == 0) { poll_flags(rws, lane < 16 ? (int)(F_G + (rt >> 1) * 16 + lane) : -1, tag); lds_signal(L, 10); }
    else lds_wait(L, 10);
    STAMP(14);
    constexpr int WIN = GWIN;
    u32x4 gf[WIN][2];
    const int kh = w4 & 1;
    const unsigned gs = WS_G + (4 * rt + (w4 & 2)) * (54 * 1024) + kh * 3072;
#define G_LOAD(i) do { gf[(i) % WIN][0] = ld16(rws, vo + ((i) % 3) * 1024, gs + KS5((i) / 3) * 6144); gf[(i) % WIN][1] = ld16(rws, vo + ((i) % 3) * 1024, gs + 54 * 1024 + KS5((i) / 3) * 6144); } while (0)
#pragma unroll
    for (int i = 0; i < WIN; ++i) G_LOAD(i);
    SB();
    f32x4 ac[2][5];
#pragma unroll
    for (int n = 0; n < 5; ++n) { ac[0][n] = f32x4{0.f, 0.f, 0.f, 0.f}; ac[1][n] = ac[0][n]; }
    const lptr B5 = L + kh * 15360 + vo;
    bf16x8 wf[2][5];
    bar_l();
#pragma unroll
    for (int n = 0; n < 5; ++n) wf[0][n] = lds16(B5 + n * 1024);
#pragma unroll
    for (int i = 0; i < 27; ++i) {
      if (i % 3 == 0 && i > 0 && i < 24) bar_l();
      if (i + 1 < 27) {
#pragma unroll
        for (int n = 0; n < 5; ++n) wf[(i + 1) & 1][n] = lds16(B5 + (((i + 1) / 3) % 5) * P5_STAGE + (((i + 1) % 3) * 5 + n) * 1024);
      }
#pragma unroll
      for (int n = 0; n < 5; ++n) {
        ac[0][n] = MFMA16(wf[i & 1][n], __builtin_bit_cast(bf16x8, gf[i % WIN][0]), ac[0][n]);
        ac[1][n] = MFMA16(wf[i & 1][n], __builtin_bit_cast(bf16x8, gf[i % WIN][1]), ac[1][n]);
      }
      if (i + 1 < 27) { _Pragma("unroll") for (int n = 0; n < 5; ++n) { SGB(0x008, 1); SGB(0x100, 1); SGB(0x008, 1); } }
      SB();
      if (i + WIN < 27) { G_LOAD(i + WIN); SB(); }
    }
    STAMP(15);
#pragma unroll
    for (int n = 0; n < 5; ++n) *(LAS f32x4*)(L + (w4 ^ 1) * 5120 + n * 1024 + vo) = kh ? ac[0][n] : ac[1][n];
    bar_l();
#pragma unroll
    for (int n = 0; n < 5; ++n) ac[0][n] = (kh ? ac[1][n] : ac[0][n]) + *(LAS f32x4*)(L + w4 * 5120 + n * 1024 + vo);
    asm volatile(".Lmega_tail:");
    u32x2 oq[5];
#pragma unroll
    for (int n = 0; n < 5; ++n) oq[n] = u32x2{cvtpk(ac[0][n][0] + bflo(hreg[n].x), ac[0][n][1] + bfhi(hreg[n].x)), cvtpk(ac[0][n][2] + bflo(hreg[n].y), ac[0][n][3] + bfhi(hreg[n].y))};
    u16* op = out + (size_t)(16 * T5 + r) * D_ + 80 * ct + 16 * (g & 1) + 8 * (g >> 1);
    *(u32x4*)op = pair16(oq[0], oq[1]);
    *(u32x4*)(op + 32) = pair16(oq[2], oq[3]);
    const u32x4 last = pair16(oq[4], oq[4]);
    if (!(g & 1)) *(u32x4*)(op + 64) = last;
    STAMP(16);
  }
}

extern "C" void kernel_launch(void* const* d_in, const int* in_sizes, int n_in, void* d_out, int out_size, void* d_ws, size_t ws_size,
                              hipStream_t stream) {
  (void)hipFuncSetAttribute((const void*)mega, hipFuncAttributeMaxDynamicSharedMemorySize, LDS_BYTES);
  mega<<<256, 512, LDS_BYTES, stream>>>((const u16*)d_in[0], (const u16*)d_in[1], (const u16*)d_in[2], (const u16*)d_in[4], (const u16*)d_in[5],
                                        (const u16*)d_in[6], (u16*)d_out, (char*)d_ws);
}
```

```cpp
#include <hip/hip_runtime.h>
#include <hip/hip_bf16.h>
#include <stdint.h>

#define LAS __attribute__((address_space(3)))
#define GAS __attribute__((address_space(1)))
typedef short bf16x8 __attribute__((ext_vector_type(8)));
typedef short s16x4 __attribute__((ext_vector_type(4)));
typedef __bf16 bf16x2 __attribute__((ext_vector_type(2)));
typedef float f32x2 __attribute__((ext_vector_type(2)));
typedef float f32x4 __attribute__((ext_vector_type(4)));
typedef float f32x16 __attribute__((ext_vector_type(16)));
typedef unsigned u32x2 __attribute__((ext_vector_type(2)));
typedef unsigned u32x4 __attribute__((ext_vector_type(4)));
typedef unsigned long long u64;
typedef unsigned short u16;
typedef LAS char* lptr;

constexpr int D_ = 640;
constexpr float LN_EPS = 1e-5f;
constexpr float QSCALE = 0.22360679774997896f * 1.4426950408889634f;

constexpr unsigned WS_FLAG = 0;
constexpr unsigned F_Q = 0, F_RT = 256, F_O = 512, F_H = 768, F_G = 1024, F_S = 1280, F_EPOCH = 1536;
constexpr unsigned WS_QKV = 262144;
constexpr unsigned WS_O   = WS_QKV + 3 * 32 * 128 * 640;
constexpr unsigned WS_H   = WS_O + 128 * 20 * 1024;
constexpr unsigned WS_G   = WS_H + 128 * 20 * 1024;
constexpr unsigned WS_WO  = WS_G + 128 * 54 * 1024;
constexpr unsigned WS_WD  = WS_WO + 8 * 20 * 5 * 1024;
constexpr unsigned WS_WGU = WS_WD + 8 * 54 * 5 * 1024;
constexpr unsigned WS_HST = WS_WGU + 20 * 108 * 2 * 1024 + 4096;
constexpr unsigned WS_CS  = WS_HST + 128 * 8 * 16 * 8;
constexpr unsigned WS_S1  = WS_CS + 4 * 2 * 1728 * 4 + 4096;
constexpr unsigned WS_C1  = WS_S1 + 2048 * 8;
constexpr unsigned WS_END = WS_C1 + 15 * 16 * 512;

constexpr int LDS_BYTES = 163840;
constexpr int L_MISC = 161792;
constexpr int L_CNT = LDS_BYTES - 64;
constexpr int P1_STAGE = 32768, L_RT = 3 * P1_STAGE, L_RTCS = 159744;
constexpr int A_K = 0, A_V = 20480, A_Q = 40960, A_ONE = 51200, L_WO = 57344;
constexpr int P4_W = 28672, P4_A0 = 3 * P4_W, P4_A = 16384;
constexpr int P5_STAGE = 30720;

#define STAMP(i) do {} while (0)

#define ROT 0
#define P5_PRE 3
#define P1_ROT (ROT ? ((idx >> 2) + 2 * (idx & 3)) % 10 : 0)
__device__ __forceinline__ unsigned cvtpk(float lo, float hi) { return __builtin_bit_cast(unsigned, __builtin_convertvector(f32x2{lo, hi}, bf16x2)); }
__device__ __forceinline__ float bflo(unsigned v) { return __uint_as_float(v << 16); }
__device__ __forceinline__ float bfhi(unsigned v) { return __uint_as_float(v & 0xffff0000u); }
__device__ __forceinline__ s16x4 trread(lptr p) { return __builtin_amdgcn_ds_read_tr16_b64_v4i16((LAS s16x4*)p); }
__device__ __forceinline__ bf16x8 cat8(s16x4 a, s16x4 b) { return __builtin_shufflevector(a, b, 0, 1, 2, 3, 4, 5, 6, 7); }
__device__ __forceinline__ bf16x8 lds16(lptr p) { return *(LAS bf16x8*)p; }
#define MFMA16(a, b, c) __builtin_amdgcn_mfma_f32_16x16x32_bf16(a, b, c, 0, 0, 0)
#define MFMA32(a, b, c) __builtin_amdgcn_mfma_f32_32x32x16_bf16(a, b, c, 0, 0, 0)
#define MFMA32K8(a, b, c) __builtin_amdgcn_mfma_f32_32x32x8bf16_1k(a, b, c, 0, 0, 0)
#define SB() __builtin_amdgcn_sched_barrier(0)
#define SGB(mask, n) __builtin_amdgcn_sched_group_barrier(mask, n, 0)
__device__ __forceinline__ unsigned rfl(unsigned v) { return __builtin_amdgcn_readfirstlane(v); }
template <int CTRL> __device__ __forceinline__ float dppf(float v) { return __uint_as_float(__builtin_amdgcn_update_dpp(0u, __float_as_uint(v), CTRL, 0xf, 0xf, true)); }
__device__ __forceinline__ float add16(float v) { const auto t = __builtin_amdgcn_permlane16_swap(__float_as_uint(v), __float_as_uint(v), false, false); return __uint_as_float(t[0]) + __uint_as_float(t[1]); }
__device__ __forceinline__ float add32(float v) { const auto t = __builtin_amdgcn_permlane32_swap(__float_as_uint(v), __float_as_uint(v), false, false); return __uint_as_float(t[0]) + __uint_as_float(t[1]); }
__device__ __forceinline__ float sum32(float v) { v += dppf<0xB1>(v); v += dppf<0x4E>(v); v += dppf<0x141>(v); v += dppf<0x140>(v); return add16(v); }
__device__ __forceinline__ float bfr(float v) { return bflo(cvtpk(v, v)); }
__device__ __forceinline__ u32x4 mkdesc(const void* p, unsigned bytes) { const u64 a = (u64)p; return u32x4{(unsigned)a, (unsigned)(a >> 32) & 0xffffu, bytes, 0x00020000u}; }
template <int N> __device__ __forceinline__ void waitv() { asm volatile("s_waitcnt vmcnt(%0)" :: "i"(N) : "memory"); }
template <int N> __device__ __forceinline__ void waitv_bar() { SB(); asm volatile("s_waitcnt vmcnt(%0) lgkmcnt(0)\n\ts_barrier" :: "i"(N) : "memory"); SB(); }
__device__ __forceinline__ void bar_l() { SB(); asm volatile("s_waitcnt lgkmcnt(0)\n\ts_barrier" ::: "memory"); SB(); }

#define DMA1(o, sc) "buffer_load_dwordx4 %0, %1, %2 offen offset:" #o sc " lds\n\t"
template <int N, bool SC1> __device__ __forceinline__ void dma_run(unsigned voff, u32x4 d, unsigned soff, unsigned lds) {
#define DMA_ASM(body) asm volatile("s_mov_b32 m0, %3\n\ts_nop 0\n\t" body :: "v"(voff), "s"(d), "s"(soff), "s"(lds) : "memory")
  if constexpr (SC1) {
    if constexpr (N == 1) DMA_ASM(DMA1(0, " sc1"));
    if constexpr (N == 2) DMA_ASM(DMA1(0, " sc1") DMA1(1024, " sc1"));
    if constexpr (N == 3) DMA_ASM(DMA1(0, " sc1") DMA1(1024, " sc1") DMA1(2048, " sc1"));
    if constexpr (N == 4) DMA_ASM(DMA1(0, " sc1") DMA1(1024, " sc1") DMA1(2048, " sc1") DMA1(3072, " sc1"));
  } else {
    if constexpr (N == 1) DMA_ASM(DMA1(0, ""));
    if constexpr (N == 2) DMA_ASM(DMA1(0, "") DMA1(1024, ""));
    if constexpr (N == 3) DMA_ASM(DMA1(0, "") DMA1(1024, "") DMA1(2048, ""));
    if constexpr (N == 4) DMA_ASM(DMA1(0, "") DMA1(1024, "") DMA1(2048, "") DMA1(3072, ""));
  }
#undef DMA_ASM
}
__device__ __forceinline__ void dma4v(const unsigned (&v)[4], u32x4 d, unsigned soff, unsigned lds) {
  asm volatile("s_mov_b32 m0, %6\n\ts_nop 0\n\t"
               "buffer_load_dwordx4 %0, %4, %5 offen lds\n\t"
               "buffer_load_dwordx4 %1, %4, %5 offen offset:1024 lds\n\t"
               "buffer_load_dwordx4 %2, %4, %5 offen offset:2048 lds\n\t"
               "buffer_load_dwordx4 %3, %4, %5 offen offset:3072 lds"
               :: "v"(v[0]), "v"(v[1]), "v"(v[2]), "v"(v[3]), "s"(d), "s"(soff), "s"(lds) : "memory");
}

typedef __amdgpu_buffer_rsrc_t rsrc_t;
__device__ __forceinline__ void st8(rsrc_t r, unsigned voff, unsigned soff, u32x2 v) { __builtin_amdgcn_raw_buffer_store_b64(v, r, voff, soff, 16); }
__device__ __forceinline__ void st16(rsrc_t r, unsigned voff, unsigned soff, u32x4 v) { __builtin_amdgcn_raw_buffer_store_b128(v, r, voff + soff, 0, 16); }
__device__ __forceinline__ void st4(rsrc_t r, unsigned voff, unsigned soff, unsigned v) { __builtin_amdgcn_raw_buffer_store_b32(v, r, voff, soff, 16); }
__device__ __forceinline__ u32x4 ld16(rsrc_t r, unsigned voff, unsigned soff) { return __builtin_amdgcn_raw_buffer_load_b128(r, voff, soff, 16); }
__device__ __forceinline__ u32x2 ld8(rsrc_t r, unsigned voff, unsigned soff) { return __builtin_amdgcn_raw_buffer_load_b64(r, voff, soff, 16); }
__device__ __forceinline__ unsigned ld4(rsrc_t r, unsigned voff, unsigned soff) { return __builtin_amdgcn_raw_buffer_load_b32(r, voff, soff, 16); }

__device__ __forceinline__ u32x2 flagval(unsigned tag) { return u32x2{tag, tag * 0x9E3779B1u + 0x7F4A7C15u}; }
__device__ __forceinline__ void poll_flags(rsrc_t r, int fidx, unsigned tag) {
  const u32x2 want = flagval(tag);
  const unsigned off = (unsigned)(fidx < 0 ? 0 : fidx) * 128u;
#pragma unroll 1
  for (int spin = 0; spin < (1 << 16); ++spin) {
    const u32x2 v = ld8(r, off, WS_FLAG);
    if (__all(fidx < 0 || (v.x == want.x && v.y == want.y))) break;
    __builtin_amdgcn_s_sleep(1);
  }
  __builtin_amdgcn_fence(__ATOMIC_ACQUIRE, "wavefront");
  asm volatile("" ::: "memory");
}
__device__ __forceinline__ bool arrive_last(lptr L, int cnt, unsigned n) {
  asm volatile("s_waitcnt vmcnt(0) lgkmcnt(0)" ::: "memory");
  unsigned old = 0;
  if ((threadIdx.x & 63) == 0) old = __hip_atomic_fetch_add((LAS unsigned*)(L + L_CNT + 4 * cnt), 1u, __ATOMIC_RELAXED, __HIP_MEMORY_SCOPE_WORKGROUP);
  old = rfl(old);
  asm volatile("" ::: "memory");
  return old == n - 1;
}
__device__ __forceinline__ void group_sync(lptr L, int cnt, unsigned n) {
  asm volatile("s_waitcnt lgkmcnt(0)" ::: "memory");
  if ((threadIdx.x & 63) == 0) __hip_atomic_fetch_add((LAS unsigned*)(L + L_CNT + 4 * cnt), 1u, __ATOMIC_RELAXED, __HIP_MEMORY_SCOPE_WORKGROUP);
#pragma unroll 1
  for (int spin = 0; spin < (1 << 22); ++spin) { if (rfl(*(volatile LAS unsigned*)(L + L_CNT + 4 * cnt)) >= n) break; }
  asm volatile("" ::: "memory");
}
__device__ __forceinline__ void raise_flag(rsrc_t r, unsigned fidx, unsigned tag) { if ((threadIdx.x & 63) == 0) st8(r, 0, WS_FLAG + fidx * 128u, flagval(tag)); }
__device__ __forceinline__ void lds_signal(lptr L, int cnt) { asm volatile("" ::: "memory"); if ((threadIdx.x & 63) == 0) *(volatile LAS unsigned*)(L + L_CNT + 4 * cnt) = 1u; }
__device__ __forceinline__ void lds_wait(lptr L, int cnt) {
#pragma unroll 1
  for (int spin = 0; spin < (1 << 22); ++spin) { if (rfl(*(volatile LAS unsigned*)(L + L_CNT + 4 * cnt))) break; __builtin_amdgcn_s_sleep(2); }
  asm volatile("" ::: "memory");
}

__device__ __forceinline__ u32x4 pair16(u32x2 a, u32x2 b) {
  const auto x = __builtin_amdgcn_permlane16_swap(a.x, b.x, false, false), y = __builtin_amdgcn_permlane16_swap(a.y, b.y, false, false);
  return u32x4{x[0], y[0], x[1], y[1]};
}
__device__ __forceinline__ void retile_read(lptr u, int lane, u32x4 (&f)[4]) {
  const lptr rp = u + (8 * (lane >> 4) + ((lane & 15) >> 2)) * 128 + (lane & 3) * 8;
#pragma unroll
  for (int n = 0; n < 4; ++n) f[n] = __builtin_bit_cast(u32x4, cat8(trread(rp + n * 32), trread(rp + n * 32 + 512)));
}
__device__ __forceinline__ void colsum_out(const u32x4 (&f)[4], lptr dst, int lane) {
#pragma unroll
  for (int n = 0; n < 4; ++n) {
    float c = ((bflo(f[n].x) + bfhi(f[n].x)) + (bflo(f[n].y) + bfhi(f[n].y))) + ((bflo(f[n].z) + bfhi(f[n].z)) + (bflo(f[n].w) + bfhi(f[n].w)));
    c = add32(add16(c));
    if (lane < 16) *(LAS float*)(dst + (n * 16 + lane) * 4) = c;
  }
}

__global__ __launch_bounds__(512) __attribute__((amdgpu_waves_per_eu(2, 2)))
void mega(const u16* __restrict__ x, const u16* __restrict__ wqkv, const u16* __restrict__ wo, const u16* __restrict__ wg, const u16* __restrict__ wu,
          const u16* __restrict__ wd, u16* __restrict__ out, char* __restrict__ ws) {
  extern __shared__ __attribute__((aligned(16))) char smem[];
  const lptr L = (lptr)smem;
  const unsigned lds0 = (unsigned)(uintptr_t)L;
  const int bid = blockIdx.x, xcd = bid & 7, idx = bid >> 3;
  const int tid = threadIdx.x, lane = tid & 63, wid = rfl(tid >> 6), w4 = wid & 3;
  const bool isC = wid < 4;
  const int r = lane & 15, g = lane >> 4;
  const unsigned vo = lane * 16;
  const rsrc_t rws = __builtin_amdgcn_make_buffer_rsrc(ws, 0, WS_END, 0x00020000);
  const u32x4 dws = mkdesc(ws, WS_END);
  STAMP(0);
  unsigned epoch_v;
  asm volatile("buffer_load_dword %0, off, %1, 0 offset:0 sc1" : "=v"(epoch_v) : "s"(mkdesc(ws + WS_FLAG + F_EPOCH * 128, 128)) : "memory");
  if (tid < 16) *(LAS unsigned*)(L + L_CNT + 4 * tid) = 0u;
  unsigned tag;
  int rid;

  const int tm = 4 * (xcd >> 1) + (idx & 3), tn = (8 * (xcd & 1) + (idx >> 2)) ^ (idx >= 30 ? 8 : 0);
  LAS f32x2* st_tok = (LAS f32x2*)(L + L_MISC);
  LAS float* st_col = (LAS float*)(L + L_MISC + 1024);
  if (isC) {
    const int row = 128 * tm + 8 * tn + 2 * w4 + (lane >> 5);
    u32x2 xv[5], wv[5];
#pragma unroll
    for (int i = 0; i < 5; ++i) xv[i] = *(const u32x2*)(x + row * D_ + ((lane & 31) + 32 * i) * 4);
#pragma unroll
    for (int i = 0; i < 5; ++i) wv[i] = *(const u32x2*)(wqkv + (40 * tm + 10 * w4 + 2 * i + (lane >> 5)) * 1920 + 128 * (tn < 15 ? tn : 0) + (lane & 31) * 4);
    bar_l();
    float s1 = 0.f, s2 = 0.f;
#pragma unroll
    for (int i = 0; i < 5; ++i) s1 += (bflo(xv[i].x) + bfhi(xv[i].x)) + (bflo(xv[i].y) + bfhi(xv[i].y));
    const float mean = sum32(s1) * (1.0f / D_);
#pragma unroll
    for (int i = 0; i < 5; ++i) { const float a = bflo(xv[i].x) - mean, b = bfhi(xv[i].x) - mean, c = bflo(xv[i].y) - mean, d = bfhi(xv[i].y) - mean; s2 += (a * a + b * b) + (c * c + d * d); }
    const float sd = bfr(__builtin_sqrtf(bfr(bfr(sum32(s2) * (1.0f / D_)) + LN_EPS)));
    if ((lane & 31) == 0) st8(rws, row * 8, WS_S1, u32x2{__float_as_uint(bfr(mean)), __float_as_uint(1.0f / sd)});
    f32x4 cs = {0.f, 0.f, 0.f, 0.f};
#pragma unroll
    for (int i = 0; i < 5; ++i) cs += f32x4{bflo(wv[i].x), bfhi(wv[i].x), bflo(wv[i].y), bfhi(wv[i].y)};
#pragma unroll
    for (int e = 0; e < 4; ++e) cs[e] = add32(cs[e]);
    if (lane < 32) *(LAS f32x4*)(L + L_RTCS + w4 * 512 + lane * 16) = cs;
    asm volatile("" : "+v"(epoch_v) : "v"(sd));
    tag = rfl(epoch_v) + 1u;
  } else bar_l();
  if (tn < 15) {
    const int aid = tm * 15 + tn;
    rid = aid;
    if (!isC) {
      unsigned voffX[4], voffW[4];
#pragma unroll
      for (int i = 0; i < 4; ++i) {
        const int p = w4 * 4 + i;
        voffX[i] = (unsigned)((8 * p + (lane >> 3)) * 1280 + (((lane & 7) ^ (lane >> 3)) * 16) - 1024 * i);
        const int f = (lane >> 4) | (((p >> 1) & 1) << 2), p16 = lane & 15;
        voffW[i] = (unsigned)((4 * p + (lane >> 4)) * 3840 + (((((p16 >> 1) ^ f) << 1) | (p16 & 1)) * 16) - 1024 * i);
      }
      const u32x4 dx = mkdesc(x, 2048 * 1280), dw = mkdesc(wqkv, 640 * 3840);
      const unsigned xs0 = tm * 128 * 1280, ws0 = tn * 256, ld0 = lds0 + w4 * 4096;
      int ks = P1_ROT;
      dma4v(voffX, dx, xs0 + ks * 128, ld0); dma4v(voffW, dw, ws0 + ks * (64 * 3840), ld0 + 16384);
      int slot = 1;
#pragma unroll 1
      for (int st = 1; st < 9; ++st) {
        ks = ks == 9 ? 0 : ks + 1;
        dma4v(voffX, dx, xs0 + ks * 128, ld0 + slot * P1_STAGE); dma4v(voffW, dw, ws0 + ks * (64 * 3840), ld0 + slot * P1_STAGE + 16384);
        slot = slot == 2 ? 0 : slot + 1;
        waitv_bar<8>();
      }
      asm volatile("" : "+v"(epoch_v));
      tag = rfl(epoch_v) + 1u;
      const int fs = lane < 16 ? F_S + tm * 16 + lane : lane < 32 ? F_S + (lane - 16) * 16 + tn : -1;
      u32x2 fv;
      asm volatile("buffer_load_dwordx2 %0, %1, %2, 0 offen sc1" : "=v"(fv) : "v"((unsigned)(fs < 0 ? 0 : fs) * 128u), "s"(dws) : "memory");
      ks = ks == 9 ? 0 : ks + 1;
      dma4v(voffX, dx, xs0 + ks * 128, ld0); dma4v(voffW, dw, ws0 + ks * (64 * 3840), ld0 + 16384);
      asm volatile("s_waitcnt vmcnt(8) lgkmcnt(0)\n\ts_barrier" : "+v"(fv) :: "memory");
      { const u32x2 want = flagval(tag); if (!__all(fs < 0 || (fv.x == want.x && fv.y == want.y))) poll_flags(rws, fs, tag); }
      const u32x4 c0 = ld16(rws, (lane >> 3) * 512 + (lane & 7) * 16, WS_C1 + tn * 8192 + w4 * 128), c1 = ld16(rws, (lane >> 3) * 512 + (lane & 7) * 16, WS_C1 + tn * 8192 + 4096 + w4 * 128);
      if (w4 == 0) dma_run<1, true>(vo, dws, WS_S1 + tm * 1024, lds0 + L_MISC);
      waitv_bar<0>();
      f32x4 cs = __builtin_bit_cast(f32x4, c0) + __builtin_bit_cast(f32x4, c1);
#pragma unroll
      for (int e = 0; e < 4; ++e) cs[e] = add32(add16(cs[e] + dppf<0x128>(cs[e])));
      if (lane < 8) *(LAS f32x4*)(st_col + w4 * 32 + lane * 4) = cs;
      const bool gu_ok = aid < 216, d_ok = aid < 180;
      const int cb = (gu_ok ? aid : 0) >> 2, isup = cb >= 27, cbb = cb - 27 * isup;
      const u32x4 dgu = mkdesc(isup ? wu : wg, 640 * 3456), dd = mkdesc(wd, 1728 * 1280);
      unsigned vgu[4], vd[4];
#pragma unroll
      for (int i = 0; i < 4; ++i) { vgu[i] = (8 * i + (lane >> 3)) * 3456 + (lane & 7) * 16 - 1024 * i; vd[i] = (8 * i + (lane >> 3)) * 1280 + (lane & 7) * 16 - 1024 * i; }
      const int sA = 5 * (aid & 3) + w4, sB = 5 * (aid & 3) + 4;
      const int ud = (d_ok ? aid : 0) * 3 + (w4 > 0 ? w4 - 1 : 0), dcb = ud / 54, dsx = ud - 54 * dcb;
      dma4v(vgu, dgu, sA * (32 * 3456) + cbb * 128, lds0 + L_RT + w4 * 4096);
      if (w4 == 0) dma4v(vgu, dgu, sB * (32 * 3456) + cbb * 128, lds0 + L_RT + 16384);
      else dma4v(vd, dd, dsx * (32 * 1280) + dcb * 128, lds0 + L_RT + 16384 + w4 * 4096);
      bar_l();
      waitv<0>();
      u32x4 rtf[2][4];
      retile_read(L + L_RT + w4 * 4096, lane, rtf[0]);
      retile_read(L + L_RT + 16384 + w4 * 4096, lane, rtf[1]);
      colsum_out(rtf[0], L + L_RTCS + w4 * 256, lane);
      if (w4 == 0) colsum_out(rtf[1], L + L_RTCS + 1024, lane);
      group_sync(L, 6, 4);
      lds_wait(L, 7);
#pragma unroll
      for (int n = 0; n < 4; ++n) {
        if (gu_ok) st16(rws, vo, WS_WGU + ((sA * 108 + 4 * cbb + n) * 2 + isup) * 1024, rtf[0][n]);
        const int t = 4 * dcb + n, sl = t / 5;
        if (w4 == 0) { if (gu_ok) st16(rws, vo, WS_WGU + ((sB * 108 + 4 * cbb + n) * 2 + isup) * 1024, rtf[1][n]); }
        else if (d_ok) st16(rws, vo, WS_WD + ((sl * 54 + dsx) * 5 + (t - 5 * sl)) * 1024, rtf[1][n]);
      }
      if (w4 == 1 && gu_ok) {
        float t = 0.f;
#pragma unroll
        for (int w = 0; w < 5; ++w) t += *(LAS float*)(L + L_RTCS + (w * 64 + lane) * 4);
        st4(rws, lane * 4, WS_CS + ((aid & 3) * 3456 + cb * 64) * 4, __float_as_uint(t));
      }
      if (arrive_last(L, 1, 4)) raise_flag(rws, F_RT + rid, tag);
    } else {
      const int wr = w4 >> 1, wc = w4 & 1;
      const int xo = (64 * wr + r) * 128 + ((g ^ (r & 7)) * 16);
      int wo_[4];
      { const int q = r >> 2, p = lane & 3, f = q | ((g & 1) << 2);
#pragma unroll
        for (int n = 0; n < 4; ++n) wo_[n] = 16384 + (8 * g + q) * 256 + (((4 * wc + n) ^ f) * 32) + p * 8; }
      f32x4 acc[4][4];
#pragma unroll
      for (int n = 0; n < 4; ++n)
#pragma unroll
        for (int m = 0; m < 4; ++m) acc[n][m] = f32x4{0.f, 0.f, 0.f, 0.f};
      bf16x8 xa[4], wa[4], xb[4], wb[4];
#define P1_READ(xf, wf, B, kk) do { \
        xf[0] = lds16(B + (xo ^ (64 * kk))); \
        wf[0] = cat8(trread(B + wo_[0] + 8192 * kk), trread(B + wo_[0] + 8192 * kk + 1024)); \
        _Pragma("unroll") for (int m = 1; m < 4; ++m) xf[m] = lds16(B + (xo ^ (64 * kk)) + 2048 * m); \
        _Pragma("unroll") for (int n = 1; n < 4; ++n) wf[n] = cat8(trread(B + wo_[n] + 8192 * kk), trread(B + wo_[n] + 8192 * kk + 1024)); } while (0)
#define P1_MMA(xf, wf) do { \
        _Pragma("unroll") for (int n = 0; n < 4; ++n) _Pragma("unroll") for (int m = 0; m < 4; ++m) acc[n][m] = MFMA16(wf[n], xf[m], acc[n][m]); \
        _Pragma("unroll") for (int i = 0; i < 12; ++i) { SGB(0x008, 1); SGB(0x100, 1); } SGB(0x008, 4); } while (0)
      bar_l();
      STAMP(17);
      P1_READ(xa, wa, L, 0);
      if (wid == 0 && lane < 32) {
        const lptr cp = L + L_RTCS + lane * 16;
        st16(rws, vo, WS_C1 + (tn * 16 + tm) * 512, __builtin_bit_cast(u32x4, (*(LAS f32x4*)cp + *(LAS f32x4*)(cp + 512)) + (*(LAS f32x4*)(cp + 1024) + *(LAS f32x4*)(cp + 1536))));
      }
      SB();
      int slot = 0;
#pragma unroll 1
      for (int st = 0; st < 10; ++st) {
        const lptr B = L + slot * P1_STAGE;
        slot = slot == 2 ? 0 : slot + 1;
        const lptr Bn = L + slot * P1_STAGE;
        P1_READ(xb, wb, B, 1);
        P1_MMA(xa, wa);
        if (st == 1) { waitv_bar<0>(); if (wid == 0) raise_flag(rws, F_S + tm * 16 + tn, tag); SB(); }
        else bar_l();
        P1_READ(xa, wa, Bn, 0);
        P1_MMA(xb, wb);
        SB();
      }
      STAMP(1);
      const int tensor = tn / 5;
      const float osc = tensor == 0 ? QSCALE : 1.0f;
      f32x2 ts[4];
#pragma unroll
      for (int m = 0; m < 4; ++m) { ts[m] = st_tok[64 * wr + 16 * m + r]; ts[m].y *= osc; }
      const unsigned so = WS_QKV + (tensor * 32 * 64 + 4 * tm + 2 * wr) * 1280;
#pragma unroll
      for (int n = 0; n < 4; ++n) {
        const f32x4 cs = *(LAS f32x4*)(st_col + 64 * wc + 16 * n + 4 * g);
        const int c4 = (tn - 5 * tensor) * 32 + 16 * wc + 4 * n + g;
        const int head = c4 / 5, dg = c4 - 5 * head;
        const unsigned dv = head * (64 * 1280) + (r >> 2) * 320 + dg * 64 + (r & 3) * 16;
#pragma unroll
        for (int m = 0; m < 4; m += 2) {
          const f32x4 v = (acc[n][m] - ts[m].x * cs) * ts[m].y, w = (acc[n][m + 1] - ts[m + 1].x * cs) * ts[m + 1].y;
          st16(rws, dv, so + (m >> 1) * 1280, u32x4{cvtpk(v[0], v[1]), cvtpk(v[2], v[3]), cvtpk(w[0], w[1]), cvtpk(w[2], w[3])});
        }
      }
      STAMP(2);
      if (arrive_last(L, 0, 4)) { raise_flag(rws, F_Q + tm * 16 + tn, tag); lds_signal(L, 7); }
    }
  } else {
    rid = 240 + tm;
    const u32x4 dwo = mkdesc(wo, 640 * 1280);
    const int u0 = tm * 8 + wid;
    unsigned rtd[2][4];
#pragma unroll
    for (int k = 0; k < 2; ++k) {
      const int u = (k == 1 && u0 + 128 < 200) ? u0 + 128 : u0, s0 = u / 10, c0 = u - 10 * s0, j = lane >> 3;
      unsigned vd[4];
#pragma unroll
      for (int i = 0; i < 4; ++i) {
        const int ch = 4 * s0 + i;
        const int row = ch < 64 ? 20 * (ch >> 1) + 4 * (ch & 1) + (j & 3) + 8 * (j >> 2) : 20 * (2 * (ch - 64) + (j >> 2)) + 16 + (j & 3);
        vd[i] = row * 1280 + (lane & 7) * 16 - 1024 * i;
        const int t = 4 * c0 + i, sl = t / 5;
        rtd[k][i] = WS_WO + ((sl * 20 + s0) * 5 + (t - 5 * sl)) * 1024;
      }
      dma4v(vd, dwo, c0 * 128, lds0 + L_RT - k * 32768 + wid * 4096);
    }
    waitv_bar<0>();
    asm volatile("" : "+v"(epoch_v));
    tag = rfl(epoch_v) + 1u;
    if (wid == 0) raise_flag(rws, F_S + tm * 16 + tn, tag);
#pragma unroll
    for (int k = 0; k < 2; ++k) {
      u32x4 rtf[4];
      retile_read(L + L_RT - k * 32768 + wid * 4096, lane, rtf);
#pragma unroll
      for (int n = 0; n < 4; ++n) st16(rws, vo, rtd[k][n], rtf[n]);
    }
    if (arrive_last(L, 5, 8)) raise_flag(rws, F_RT + rid, tag);
  }
  STAMP(3);

  const int head = 4 * xcd + (idx >> 3), qb = idx & 7;
  const int rt = 4 * xcd + (idx & 3), ct = idx >> 2;
  if (isC) {
    if (wid == 0) {
      int f = -1;
      if (lane < 24) {
        const int tmi = lane / 6, wh = lane - 6 * tmi, tmm = 2 * qb - 2 + tmi, ten = wh >> 1;
        const int tnn = 5 * ten + ((20 * head + ((wh & 1) ? 19 : 0)) >> 7);
        if (tmm >= 0 && (ten > 0 || tmi >= 2)) f = F_Q + tmm * 16 + tnn;
      } else if (lane < 40) f = F_RT + 240 + (lane - 24);
      poll_flags(rws, f, tag);
      lds_signal(L, 8);
    } else lds_wait(L, 8);
    STAMP(4);
    const unsigned hb = WS_QKV + (head * 64 + 8 * qb) * 1280;
    const unsigned kv = hb + (32 * 64 * 1280) + (w4 & 1) * 5120 - ((w4 < 2 && qb > 0) ? 10240 : 0);
    if (w4 < 2) dma_run<3, true>(vo, dws, hb + w4 * 3072, lds0 + A_Q + w4 * 3072);
    else dma_run<2, true>(vo, dws, hb + 6144 + (w4 - 2) * 2048, lds0 + A_Q + 6144 + (w4 - 2) * 2048);
    dma_run<4, true>(vo, dws, kv, lds0 + A_K + w4 * 5120); dma_run<1, true>(vo, dws, kv + 4096, lds0 + A_K + w4 * 5120 + 4096);
    dma_run<4, true>(vo, dws, kv + 32 * 64 * 1280, lds0 + A_V + w4 * 5120); dma_run<1, true>(vo, dws, kv + 32 * 64 * 1280 + 4096, lds0 + A_V + w4 * 5120 + 4096);
  } else if (wid == 4) *(LAS u32x4*)(L + A_ONE + vo) = u32x4{0x00003F80u, 0u, 0x00003F80u, 0u};
  waitv_bar<5>();
  STAMP(5);
  {
    const int c = lane & 31, hh = lane >> 5;
    const int nskip = qb == 0 ? 8 - wid : 0;
    const unsigned wos = WS_WO + ct * 102400 + wid * 12288, wod = lds0 + L_WO + wid * 12288;
#define WO_PIECE(i) dma_run<1, true>(vo, dws, wos + (i) * 1024, wod + (i) * 1024)
    bf16x8 q1; u32x2 q2;
    const int ko = wid * 1280 + ((c >> 2) & 3) * 320 + (c & 3) * 16 + (c >> 4) * 8;
#define LD8(p) (*(volatile LAS s16x4*)(p))
    { const lptr qp = L + A_Q + ko;
      q1 = cat8(LD8(qp + 128 * hh), LD8(qp + 128 * hh + 64));
      q2 = __builtin_bit_cast(u32x2, LD8(qp + 256)); if (hh) q2 = u32x2{0x3F80u, 0u}; }
    const lptr kp = L + A_K + ko;
    f32x16 s[9];
    const f32x16 zero16 = {0.f,0.f,0.f,0.f,0.f,0.f,0.f,0.f,0.f,0.f,0.f,0.f,0.f,0.f,0.f,0.f};
    bf16x8 k1[9]; u32x2 k2[9];
#pragma unroll
    for (int jj = 0; jj < 9; ++jj) {
      const int jb = jj == 0 ? 0 : jj == 1 ? 8 : jj - 1;
      k1[jb] = cat8(LD8(kp + jb * 1280 + 128 * hh), LD8(kp + jb * 1280 + 128 * hh + 64));
      k2[jb] = __builtin_bit_cast(u32x2, LD8(kp + jb * 1280 + 256));
    }
    SB();
    float m0, m1;
#pragma unroll
    for (int jj = 0; jj < 11; ++jj) {
      if (jj < 9) {
        const int jb = jj == 0 ? 0 : jj == 1 ? 8 : jj - 1;
        k2[jb].x = hh ? (jb < nskip ? 0xFF7Fu : 0u) : k2[jb].x;
        s[jb] = MFMA32(k1[jb], q1, zero16);
        s[jb] = MFMA32K8(__builtin_bit_cast(s16x4, k2[jb]), __builtin_bit_cast(s16x4, q2), s[jb]);
      }
      if (jj == 3) {
#pragma unroll
        for (int e = 0; e < 16; ++e) { const int kk = (e & 3) + 8 * (e >> 2) + 4 * hh; s[0][e] = kk > c ? s[0][e] : s[8][e]; }
        m0 = fmaxf(s[0][0], s[0][1]); m1 = fmaxf(s[0][2], s[0][3]);
#pragma unroll
        for (int e = 4; e < 16; e += 4) { m0 = fmaxf(fmaxf(m0, s[0][e]), s[0][e + 1]); m1 = fmaxf(fmaxf(m1, s[0][e + 2]), s[0][e + 3]); }
      }
      if (jj >= 4) {
        const int jb = jj - 3;
#pragma unroll
        for (int e = 0; e < 16; e += 4) { m0 = fmaxf(fmaxf(m0, s[jb][e]), s[jb][e + 1]); m1 = fmaxf(fmaxf(m1, s[jb][e + 2]), s[jb][e + 3]); }
      }
      if (jj & 1) WO_PIECE(jj >> 1);
      SB();
    }
    float mx = fmaxf(m0, m1);
    { auto rr = __builtin_amdgcn_permlane32_swap(__float_as_uint(mx), __float_as_uint(mx), false, false);
      mx = fmaxf(__uint_as_float(rr[0]), __uint_as_float(rr[1])); }
    STAMP(28);
    waitv_bar<5>();
    STAMP(29);
    const int gq = r >> 2, gp = lane & 3, up16 = (lane >> 4) & 1;
    const bool vconst = up16 && gp > 0;
    const lptr vb0 = L + (vconst ? A_ONE + 8 + gq * 16 : A_V + wid * 1280 + hh * 320 + (4 * up16 + gp) * 64 + gq * 16);
    const int vstep = vconst ? 0 : 1280;
    f32x16 o = zero16;
    bf16x8 va[2], vb[2], vc, vd;
#define V_READ(a, b, jb) do { const lptr v0 = vb0 + (jb) * vstep; a = cat8(trread(v0), trread(v0 + 640)); b = cat8(trread(v0 + 8), trread(v0 + 648)); } while (0)
    u32x4 pa[2], pb[2];
#pragma unroll
    for (int it = 0; it < 9; ++it) {
      const int jb = it == 7 ? 0 : it + 1;
      if (it < 8) {
        V_READ(va[it & 1], vb[it & 1], jb);
        if (it == 7) V_READ(vc, vd, 8);
        float p[16];
#pragma unroll
        for (int e = 0; e < 16; ++e) p[e] = __builtin_amdgcn_exp2f(s[jb][e] - mx);
        pa[it & 1] = u32x4{cvtpk(p[0], p[1]), cvtpk(p[2], p[3]), cvtpk(p[4], p[5]), cvtpk(p[6], p[7])};
        pb[it & 1] = u32x4{cvtpk(p[8], p[9]), cvtpk(p[10], p[11]), cvtpk(p[12], p[13]), cvtpk(p[14], p[15])};
      }
      if (it >= 1 && it < 8) {
        o = MFMA32(va[(it - 1) & 1], __builtin_bit_cast(bf16x8, pa[(it - 1) & 1]), o);
        o = MFMA32(vb[(it - 1) & 1], __builtin_bit_cast(bf16x8, pb[(it - 1) & 1]), o);
        SGB(0x100, 4); SGB(0x002, 4); SGB(0x008, 1); SGB(0x002, 16); SGB(0x008, 1);
      }
      if (it == 8) {
        u32x4 ma, mb;
#pragma unroll
        for (int e = 0; e < 4; ++e) {
          const int ka = 2 * (e & 1) + 8 * (e >> 1) + 4 * hh, kb = ka + 16;
          ma[e] = (ka > c ? 0xffffu : 0u) | (ka + 1 > c ? 0xffff0000u : 0u);
          mb[e] = (kb > c ? 0xffffu : 0u) | (kb + 1 > c ? 0xffff0000u : 0u);
        }
        o = MFMA32(va[1], __builtin_bit_cast(bf16x8, pa[1] & ma), o);
        o = MFMA32(vb[1], __builtin_bit_cast(bf16x8, pb[1] & mb), o);
        o = MFMA32(vc, __builtin_bit_cast(bf16x8, pa[1] & ~ma), o);
        o = MFMA32(vd, __builtin_bit_cast(bf16x8, pb[1] & ~mb), o);
      }
      if (it < 7) WO_PIECE(5 + it);
      if (it == 7 && isC) dma_run<1, true>(vo, dws, WS_WO + ct * 102400 + 98304 + wid * 1024, lds0 + L_WO + 98304 + wid * 1024);
      SB();
    }
    const float rl = __builtin_amdgcn_rcpf(hh ? o[8] : o[12]);
    const unsigned ob = WS_O + (16 * qb + 2 * wid) * 20480, ov = (c >> 4) * 20480 + (c & 15) * 16;
    st16(rws, ov + hh * 256, ob + head * 512, u32x4{cvtpk(o[0] * rl, o[1] * rl), cvtpk(o[2] * rl, o[3] * rl), cvtpk(o[4] * rl, o[5] * rl), cvtpk(o[6] * rl, o[7] * rl)});
    if (hh == 0) st8(rws, ov, ob + 16384 + (head >> 1) * 256 + (head & 1) * 8, u32x2{cvtpk(o[8] * rl, o[9] * rl), cvtpk(o[10] * rl, o[11] * rl)});
    STAMP(6);
  }
  waitv_bar<0>();
  if (wid == 0) raise_flag(rws, F_O + qb * 32 + head, tag);
  STAMP(7);

  const int pn = 4 * (xcd >> 1) + (idx & 3), cgp = 8 * (xcd & 1) + (idx >> 2);
  const int t0 = cgp < 12 ? 7 * cgp : 6 * cgp + 12, ntile = cgp < 12 ? 7 : 6;
  const int T5 = 4 * rt + w4;
  u32x2 hreg[5];
  const unsigned wgs = WS_WGU + t0 * 2048 + (w4 >> 1) * (108 * 2048) + (w4 & 1) * 7168;
  const int rot4 = P1_ROT;
#define KS4(st) ((st) + rot4 >= 10 ? (st) + rot4 - 10 : (st) + rot4)
  if (isC) {
    u32x2 xv[5];
#pragma unroll
    for (int n = 0; n < 5; ++n) xv[n] = *(const u32x2*)(x + (size_t)(16 * T5 + r) * D_ + 80 * ct + 16 * n + 4 * g);
    if (wid == 0) { poll_flags(rws, lane < 32 ? (int)(F_O + (rt >> 2) * 32 + lane) : -1, tag); lds_signal(L, 9); }
    else lds_wait(L, 9);
    STAMP(8);
    u32x4 af[20];
    const int rot3 = ROT ? (ct * 5) >> 3 : 0;
#pragma unroll
    for (int s = 0; s < 20; ++s) { int sg = (s >> 2) + rot3; sg = sg >= 5 ? sg - 5 : sg; af[s] = ld16(rws, vo + (s & 3) * 1024, WS_O + T5 * 20480 + sg * 4096); }
    SB();
    f32x4 acc[5];
#pragma unroll
    for (int n = 0; n < 5; ++n) acc[n] = f32x4{0.f, 0.f, 0.f, 0.f};
    bf16x8 wf[2][5];
    lptr B = L + L_WO + vo + rot3 * 20480;
#pragma unroll
    for (int n = 0; n < 5; ++n) wf[0][n] = lds16(B + n * 1024);
    SB();
#pragma unroll
    for (int s = 0; s < 20; ++s) {
      if (s + 1 < 20) {
        if (((s + 1) & 3) == 0) { int sg = ((s + 1) >> 2) + rot3; sg = sg >= 5 ? sg - 5 : sg; B = L + L_WO + vo + sg * 20480; }
#pragma unroll
        for (int n = 0; n < 5; ++n) wf[(s + 1) & 1][n] = lds16(B + (((s + 1) & 3) * 5 + n) * 1024);
      }
#pragma unroll
      for (int n = 0; n < 5; ++n) acc[n] = MFMA16(wf[s & 1][n], __builtin_bit_cast(bf16x8, af[s]), acc[n]);
      SB();
    }
    STAMP(9);
    float s1 = 0.f, s2 = 0.f;
#pragma unroll
    for (int n = 0; n < 5; ++n)
      hreg[n] = u32x2{cvtpk(acc[n][0] + bflo(xv[n].x), acc[n][1] + bfhi(xv[n].x)), cvtpk(acc[n][2] + bflo(xv[n].y), acc[n][3] + bfhi(xv[n].y))};
    { const unsigned hv = (g & 1) * 512 + (g >> 1) * 256 + r * 16, hsb = WS_H + T5 * 20480 + ct * 2560;
      st16(rws, hv, hsb, pair16(hreg[0], hreg[1]));
      st16(rws, hv, hsb + 1024, pair16(hreg[2], hreg[3]));
      const u32x4 last = pair16(hreg[4], hreg[4]);
      if (!(g & 1)) st16(rws, hv, hsb + 2048, last); }
#pragma unroll
    for (int n = 0; n < 5; ++n) {
      const float a = bflo(hreg[n].x), b = bfhi(hreg[n].x), c = bflo(hreg[n].y), d = bfhi(hreg[n].y);
      s1 += (a + b) + (c + d); s2 += (a * a + b * b) + (c * c + d * d);
    }
    s1 = add32(add16(s1)); s2 = add32(add16(s2));
    if (g == 0) st8(rws, r * 8, WS_HST + (T5 * 8 + ct) * 128, u32x2{__float_as_uint(s1), __float_as_uint(s2)});
    if (arrive_last(L, 3, 4)) raise_flag(rws, F_H + rt * 8 + ct, tag);
  } else {
    int f = -1;
    if (lane < 24) { const int b = lane >> 2, isup = b >= 3, cb = (t0 >> 2) + b - 3 * isup; if (cb <= ((t0 + ntile - 1) >> 2)) f = F_RT + (cb + 27 * isup) * 4 + (lane & 3); }
    poll_flags(rws, f, tag);
#pragma unroll
    for (int st = 0; st < 2; ++st) {
      dma_run<4, true>(vo, dws, wgs + KS4(st) * (2 * 108 * 2048), lds0 + st * P4_W + w4 * 7168);
      dma_run<3, true>(vo, dws, wgs + KS4(st) * (2 * 108 * 2048) + 4096, lds0 + st * P4_W + w4 * 7168 + 4096);
    }
    const int cc = w4 * 64 + lane;
    if (cc < 224) {
      const unsigned o = (cc >= 112 ? 1728 - 112 : 0) + cc + t0 * 16;
      const float t = (__uint_as_float(ld4(rws, o * 4, WS_CS)) + __uint_as_float(ld4(rws, o * 4, WS_CS + 3456 * 4))) +
                      (__uint_as_float(ld4(rws, o * 4, WS_CS + 2 * 3456 * 4)) + __uint_as_float(ld4(rws, o * 4, WS_CS + 3 * 3456 * 4)));
      *(LAS float*)(L + L_MISC + cc * 4) = t;
    }
#pragma unroll
    for (int n = 0; n < 5; ++n) hreg[n] = u32x2{0u, 0u};
  }
  STAMP(10);
  bar_l();

  const int T4 = 8 * pn + 2 * w4;
  const int rot5 = ROT ? ct : 0;
#define KS5(st) ((st) + rot5 >= 9 ? (st) + rot5 - 9 : (st) + rot5)
  if (!isC) {
    dma_run<4, true>(vo, dws, wgs + KS4(2) * (2 * 108 * 2048), lds0 + 2 * P4_W + w4 * 7168);
    dma_run<3, true>(vo, dws, wgs + KS4(2) * (2 * 108 * 2048) + 4096, lds0 + 2 * P4_W + w4 * 7168 + 4096);
    poll_flags(rws, lane < 8 ? (int)(F_H + (2 * pn + (w4 >> 1)) * 8 + lane) : -1, tag);
    STAMP(11);
    const unsigned as = WS_H + T4 * 20480, ad = lds0 + P4_A0 + w4 * 4096;
#pragma unroll
    for (int st = 0; st < 3; ++st) { dma_run<2, true>(vo, dws, as + KS4(st) * 2048, ad + st * P4_A); dma_run<2, true>(vo, dws, as + 20480 + KS4(st) * 2048, ad + st * P4_A + 2048); }
    waitv_bar<8>();
    waitv_bar<4>();
    int slot = 0;
#pragma unroll 1
    for (int st = 3; st < 10; ++st) {
      const int ks = KS4(st);
      dma_run<2, true>(vo, dws, as + ks * 2048, ad + slot * P4_A); dma_run<2, true>(vo, dws, as + 20480 + ks * 2048, ad + slot * P4_A + 2048);
      dma_run<4, true>(vo, dws, wgs + ks * (2 * 108 * 2048), lds0 + slot * P4_W + w4 * 7168);
      dma_run<3, true>(vo, dws, wgs + ks * (2 * 108 * 2048) + 4096, lds0 + slot * P4_W + w4 * 7168 + 4096);
      slot = slot == 2 ? 0 : slot + 1;
      waitv_bar<11>();
    }
    u32x2 hs[2][2];
#pragma unroll
    for (int m = 0; m < 2; ++m) { hs[m][0] = ld8(rws, lane * 8, WS_HST + (T4 + m) * 1024); hs[m][1] = ld8(rws, lane * 8, WS_HST + (T4 + m) * 1024 + 512); }
    waitv_bar<0>();
#pragma unroll
    for (int m = 0; m < 2; ++m) {
      const float s1 = add32(add16(__uint_as_float(hs[m][0].x) + __uint_as_float(hs[m][1].x))), s2 = add32(add16(__uint_as_float(hs[m][0].y) + __uint_as_float(hs[m][1].y)));
      const float mean = s1 * (1.0f / D_);
      if (g == 0) *(LAS f32x2*)(L + L_MISC + 896 + (w4 * 32 + m * 16 + r) * 8) = f32x2{mean, fmaxf(s2 * (1.0f / D_) - mean * mean, 0.f) + LN_EPS};
    }
    int f = -1;
    if (lane < 36) { const int b = lane / 18; f = F_RT + (((5 * ct) >> 2) + b) * 18 + (lane - 18 * b); }
    poll_flags(rws, f, tag);
    bar_l();
    STAMP(12);
    const unsigned wpo = w4 * 7168 + (w4 < 2 ? w4 : 2) * 1024, wds = WS_WD + ct * (54 * 5120) + wpo, wdd = lds0 + wpo;
#define P5_ISSUE(st, slot) do { const unsigned so_ = wds + KS5(st) * P5_STAGE, do_ = wdd + (slot) * P5_STAGE; \
      dma_run<4, true>(vo, dws, so_, do_); if (w4 < 2) dma_run<4, true>(vo, dws, so_ + 4096, do_ + 4096); else dma_run<3, true>(vo, dws, so_ + 4096, do_ + 4096); } while (0)
#pragma unroll 1
    for (int st = 0; st < 5; ++st) {
      if (st == P5_PRE) bar_l();
      P5_ISSUE(st, st);
    }
    waitv_bar<28>();
    waitv_bar<21>();
#pragma unroll 1
    for (int st = 5; st < 9; ++st) { P5_ISSUE(st, st - 5); waitv_bar<21>(); }
    waitv_bar<14>(); waitv_bar<7>(); waitv_bar<0>();
    if (bid == 0 && wid == 4) {
#pragma unroll 1
      for (int i = 0; i < 4; ++i) poll_flags(rws, F_RT + i * 64 + lane, tag);
      if (lane == 0) st4(rws, 0, WS_FLAG + F_EPOCH * 128, tag);
    }
  } else {
    f32x4 acc[7][2][2];
#pragma unroll
    for (int n = 0; n < 7; ++n)
#pragma unroll
      for (int u = 0; u < 2; ++u) { acc[n][u][0] = f32x4{0.f, 0.f, 0.f, 0.f}; acc[n][u][1] = acc[n][u][0]; }
    bf16x8 a0[2], a1[2], w0[7], w1[7];
#define P4_RW(wf, W, kk, half) do { _Pragma("unroll") for (int i = 0; i < 7; ++i) wf[i] = lds16(W + (kk) * 14336 + ((half) * 7 + i) * 1024); } while (0)
#define P4_RA(af, A, kk) do { af[0] = lds16(A + (kk) * 1024); af[1] = lds16(A + 2048 + (kk) * 1024); } while (0)
#define P4_MMA(wf, af, half, nrd) do { _Pragma("unroll") for (int i = 0; i < 7; ++i) { const int t = (half) * 7 + i; \
        acc[t >> 1][t & 1][0] = MFMA16(wf[i], af[0], acc[t >> 1][t & 1][0]); acc[t >> 1][t & 1][1] = MFMA16(wf[i], af[1], acc[t >> 1][t & 1][1]); } \
        _Pragma("unroll") for (int i = 0; i < nrd; ++i) { SGB(0x008, 1); SGB(0x100, 1); } SGB(0x008, 14 - nrd); SB(); } while (0)
    bar_l();
    STAMP(11);
    P4_RA(a0, L + P4_A0 + w4 * 4096 + vo, 0); P4_RW(w0, L + vo, 0, 0);
    SB();
    int slot = 0;
#pragma unroll 1
    for (int st = 0; st < 10; ++st) {
      const lptr W = L + slot * P4_W + vo, A = L + P4_A0 + slot * P4_A + w4 * 4096 + vo;
      slot = slot == 2 ? 0 : slot + 1;
      const lptr Wn = L + slot * P4_W + vo, An = L + P4_A0 + slot * P4_A + w4 * 4096 + vo;
      P4_RW(w1, W, 0, 1);                     P4_MMA(w0, a0, 0, 7);
      P4_RA(a1, A, 1); P4_RW(w0, W, 1, 0);    P4_MMA(w1, a0, 1, 9);
      P4_RW(w1, W, 1, 1);                     P4_MMA(w0, a1, 0, 7);
      bar_l();
      P4_RA(a0, An, 0); P4_RW(w0, Wn, 0, 0);  P4_MMA(w1, a1, 1, 9);
    }
    STAMP(12);
    constexpr float C1 = -2.0f * 0.7978845608028654f * 1.4426950408889634f, C2 = C1 * 0.044715f;
    u32x2 gq[2][7];
#pragma unroll
    for (int m = 0; m < 2; ++m) {
      const f32x2 tk = *(LAS f32x2*)(L + L_MISC + 896 + (w4 * 32 + m * 16 + r) * 8);
      const float mean = tk.x, var = tk.y;
      const float sc = __builtin_amdgcn_rsqf(var), c1s = C1 * sc, c2s = C2 * sc * sc * sc;
#pragma unroll
      for (int n = 0; n < 7; ++n) {
        const f32x4 cg = *(LAS f32x4*)(L + L_MISC + (n * 16 + 4 * g) * 4), cu = *(LAS f32x4*)(L + L_MISC + (112 + n * 16 + 4 * g) * 4);
        const f32x4 a = acc[n][0][m] - mean * cg, b = acc[n][1][m] - mean * cu;
        const f32x4 z = a * (a * a * c2s + c1s);
        f32x4 ex;
#pragma unroll
        for (int e = 0; e < 4; ++e) ex[e] = __builtin_amdgcn_exp2f(z[e]);
        const f32x4 dn = ex * var + var;
        f32x4 rc;
#pragma unroll
        for (int e = 0; e < 4; ++e) rc[e] = __builtin_amdgcn_rcpf(dn[e]);
        const f32x4 o = (a * b) * rc;
        gq[m][n] = u32x2{cvtpk(o[0], o[1]), cvtpk(o[2], o[3])};
      }
    }
#pragma unroll
    for (int n = 0; n < 7; ++n) {
      const u32x4 v = pair16(gq[0][n], gq[1][n]);
      if (n < ntile) st16(rws, (g & 1) * (54 * 1024) + (g >> 1) * 256 + r * 16, WS_G + T4 * (54 * 1024) + (t0 + n) * 512, v);
    }
    STAMP(13);
    waitv_bar<0>();
    if (wid == 0) raise_flag(rws, F_G + pn * 16 + cgp, tag);

    if (wid == 0) { poll_flags(rws, lane < 16 ? (int)(F_G + (rt >> 1) * 16 + lane) : -1, tag); lds_signal(L, 10); }
    else lds_wait(L, 10);
    STAMP(14);
    constexpr int WIN = 24;
    u32x4 gf[WIN];
    const unsigned gs = WS_G + T5 * (54 * 1024);
#define G_LOAD(s) ld16(rws, vo + ((s) % 6 & 3) * 1024, gs + KS5((s) / 6) * 6144 + ((s) % 6 >> 2) * 4096)
#pragma unroll
    for (int s = 0; s < WIN; ++s) gf[s] = G_LOAD(s);
    SB();
    f32x4 ac[5];
#pragma unroll
    for (int n = 0; n < 5; ++n) ac[n] = f32x4{0.f, 0.f, 0.f, 0.f};
#pragma unroll
    for (int st = 0; st < 9; ++st) {
      bar_l();
      const lptr B = L + (st % 5) * P5_STAGE + vo;
      bf16x8 wf[2][5];
#pragma unroll
      for (int n = 0; n < 5; ++n) wf[0][n] = lds16(B + n * 1024);
#pragma unroll
      for (int kk = 0; kk < 6; ++kk) {
        const int s = 6 * st + kk;
        if (kk < 5) {
#pragma unroll
          for (int n = 0; n < 5; ++n) wf[(kk + 1) & 1][n] = lds16(B + ((kk + 1) * 5 + n) * 1024);
        }
#pragma unroll
        for (int n = 0; n < 5; ++n) ac[n] = MFMA16(wf[kk & 1][n], __builtin_bit_cast(bf16x8, gf[s % WIN]), ac[n]);
        if (s + WIN < 54) gf[s % WIN] = G_LOAD(s + WIN);
        SB();
      }
    }
    STAMP(15);
    u32x2 oq[5];
#pragma unroll
    for (int n = 0; n < 5; ++n) oq[n] = u32x2{cvtpk(ac[n][0] + bflo(hreg[n].x), ac[n][1] + bfhi(hreg[n].x)), cvtpk(ac[n][2] + bflo(hreg[n].y), ac[n][3] + bfhi(hreg[n].y))};
    u16* op = out + (size_t)(16 * T5 + r) * D_ + 80 * ct + 16 * (g & 1) + 8 * (g >> 1);
    *(u32x4*)op = pair16(oq[0], oq[1]);
    *(u32x4*)(op + 32) = pair16(oq[2], oq[3]);
    const u32x4 last = pair16(oq[4], oq[4]);
    if (!(g & 1)) *(u32x4*)(op + 64) = last;
    STAMP(16);
  }
}

extern "C" void kernel_launch(void* const* d_in, const int* in_sizes, int n_in, void* d_out, int out_size, void* d_ws, size_t ws_size,
                              hipStream_t stream) {
  (void)hipFuncSetAttribute((const void*)mega, hipFuncAttributeMaxDynamicSharedMemorySize, LDS_BYTES);
  mega<<<256, 512, LDS_BYTES, stream>>>((const u16*)d_in[0], (const u16*)d_in[1], (const u16*)d_in[2], (const u16*)d_in[4], (const u16*)d_in[5],
                                        (const u16*)d_in[6], (u16*)d_out, (char*)d_ws);
}
```

```cpp
#define PADX 0
#include <hip/hip_runtime.h>
#include <hip/hip_bf16.h>
#include <stdint.h>

#define LAS __attribute__((address_space(3)))
#define GAS __attribute__((address_space(1)))
typedef short bf16x8 __attribute__((ext_vector_type(8)));
typedef short s16x4 __attribute__((ext_vector_type(4)));
typedef __bf16 bf16x2 __attribute__((ext_vector_type(2)));
typedef float f32x2 __attribute__((ext_vector_type(2)));
typedef float f32x4 __attribute__((ext_vector_type(4)));
typedef float f32x16 __attribute__((ext_vector_type(16)));
typedef unsigned u32x2 __attribute__((ext_vector_type(2)));
typedef unsigned u32x4 __attribute__((ext_vector_type(4)));
typedef unsigned long long u64;
typedef unsigned short u16;
typedef LAS char* lptr;

constexpr int D_ = 640;
constexpr float LN_EPS = 1e-5f;
constexpr float QSCALE = 0.22360679774997896f * 1.4426950408889634f;

constexpr unsigned WS_FLAG = 0;
constexpr unsigned F_Q = 0, F_O = 256, F_H = 512, F_G = 768, F_RT = 1024, F_EPOCH = 1280;
constexpr unsigned WS_QKV = 262144;
constexpr unsigned WS_O   = WS_QKV + 3 * 32 * 128 * 640;
constexpr unsigned WS_H   = WS_O + 128 * 20 * 1024;
constexpr unsigned WS_G   = WS_H + 128 * 20 * 1024;
constexpr unsigned WS_WO  = WS_G + 128 * 54 * 1024;
constexpr unsigned WS_WD  = WS_WO + 8 * 20 * 5 * 1024;
constexpr unsigned WS_WGU = WS_WD + 8 * 54 * 5 * 1024;
constexpr unsigned WS_HST = WS_WGU + 20 * 108 * 2 * 1024 + 4096;
constexpr unsigned WS_CS  = WS_HST + 128 * 8 * 16 * 8;
constexpr unsigned WS_S1  = WS_CS + 4 * 2 * 1728 * 4 + 4096;
constexpr unsigned WS_C1  = WS_S1 + 2048 * 16;
constexpr unsigned WS_END = WS_C1 + 15 * 10 * 1024;

constexpr int LDS_BYTES = 163840;
constexpr int L_MISC = 161792;
constexpr int L_CNT = LDS_BYTES - 64;
constexpr int P1_STAGE = 32768, L_RT = 3 * P1_STAGE, L_GUCS = 159744;
constexpr int A_K = 0, A_V = 20480, A_Q = 40960, A_ONE = 51200, L_WO = 57344;
constexpr int P4_W = 28672, P4_A0 = 3 * P4_W, P4_A = 16384;
constexpr int P5_STAGE = 30720;

#define STAMP(i) do { if ((i) == PADX) padt = __builtin_amdgcn_s_memrealtime(); \
  if ((i) == 16) { while (__builtin_amdgcn_s_memrealtime() - padt < 6000ull) __builtin_amdgcn_s_sleep(1); } } while (0)

#define ROT 8
#define TOUCH 0
#define WARM 1
#define RTOUCH 1
#define DTOUCH 1
#define GWIN 12
#define LPRIO 2
#define P5_PRE 5
#define P1_ROT ((ROT & 1) ? ((idx >> 2) + 2 * (idx & 3)) % 10 : 0)
#define P4_ROT ((ROT & 4) ? ((idx >> 2) + 2 * (idx & 3)) % 10 : 0)
__device__ __forceinline__ unsigned cvtpk(float lo, float hi) { return __builtin_bit_cast(unsigned, __builtin_convertvector(f32x2{lo, hi}, bf16x2)); }
__device__ __forceinline__ float bflo(unsigned v) { return __uint_as_float(v << 16); }
__device__ __forceinline__ float bfhi(unsigned v) { return __uint_as_float(v & 0xffff0000u); }
__device__ __forceinline__ s16x4 trread(lptr p) { return __builtin_amdgcn_ds_read_tr16_b64_v4i16((LAS s16x4*)p); }
__device__ __forceinline__ bf16x8 cat8(s16x4 a, s16x4 b) { return __builtin_shufflevector(a, b, 0, 1, 2, 3, 4, 5, 6, 7); }
__device__ __forceinline__ bf16x8 lds16(lptr p) { return *(LAS bf16x8*)p; }
#define MFMA16(a, b, c) __builtin_amdgcn_mfma_f32_16x16x32_bf16(a, b, c, 0, 0, 0)
#define MFMA32(a, b, c) __builtin_amdgcn_mfma_f32_32x32x16_bf16(a, b, c, 0, 0, 0)
#define MFMA32K8(a, b, c) __builtin_amdgcn_mfma_f32_32x32x8bf16_1k(a, b, c, 0, 0, 0)
#define SB() __builtin_amdgcn_sched_barrier(0)
#define SGB(mask, n) __builtin_amdgcn_sched_group_barrier(mask, n, 0)
__device__ __forceinline__ unsigned rfl(unsigned v) { return __builtin_amdgcn_readfirstlane(v); }
template <int CTRL> __device__ __forceinline__ float dppf(float v) { return __uint_as_float(__builtin_amdgcn_update_dpp(0u, __float_as_uint(v), CTRL, 0xf, 0xf, true)); }
__device__ __forceinline__ float add16(float v) { const auto t = __builtin_amdgcn_permlane16_swap(__float_as_uint(v), __float_as_uint(v), false, false); return __uint_as_float(t[0]) + __uint_as_float(t[1]); }
__device__ __forceinline__ float add32(float v) { const auto t = __builtin_amdgcn_permlane32_swap(__float_as_uint(v), __float_as_uint(v), false, false); return __uint_as_float(t[0]) + __uint_as_float(t[1]); }
__device__ __forceinline__ float sum32(float v) { v += dppf<0xB1>(v); v += dppf<0x4E>(v); v += dppf<0x141>(v); v += dppf<0x140>(v); return add16(v); }
__device__ __forceinline__ float bfr(float v) { return bflo(cvtpk(v, v)); }
__device__ __forceinline__ u32x4 mkdesc(const void* p, unsigned bytes) { const u64 a = (u64)p; return u32x4{(unsigned)a, (unsigned)(a >> 32) & 0xffffu, bytes, 0x00020000u}; }
template <int N> __device__ __forceinline__ void waitv() { asm volatile("s_waitcnt vmcnt(%0)" :: "i"(N) : "memory"); }
template <int N> __device__ __forceinline__ void waitv_bar() { SB(); asm volatile("s_waitcnt vmcnt(%0) lgkmcnt(0)\n\ts_barrier" :: "i"(N) : "memory"); SB(); }
__device__ __forceinline__ void bar_l() { SB(); asm volatile("s_waitcnt lgkmcnt(0)\n\ts_barrier" ::: "memory"); SB(); }

#define DMA1(o, sc) "buffer_load_dwordx4 %0, %1, %2 offen offset:" #o sc " lds\n\t"
template <int N, bool SC1> __device__ __forceinline__ void dma_run(unsigned voff, u32x4 d, unsigned soff, unsigned lds) {
#define DMA_ASM(body) asm volatile("s_mov_b32 m0, %3\n\ts_nop 0\n\t" body :: "v"(voff), "s"(d), "s"(soff), "s"(lds) : "memory")
  if constexpr (SC1) {
    if constexpr (N == 1) DMA_ASM(DMA1(0, " sc1"));
    if constexpr (N == 2) DMA_ASM(DMA1(0, " sc1") DMA1(1024, " sc1"));
    if constexpr (N == 3) DMA_ASM(DMA1(0, " sc1") DMA1(1024, " sc1") DMA1(2048, " sc1"));
    if constexpr (N == 4) DMA_ASM(DMA1(0, " sc1") DMA1(1024, " sc1") DMA1(2048, " sc1") DMA1(3072, " sc1"));
  } else {
    if constexpr (N == 1) DMA_ASM(DMA1(0, ""));
    if constexpr (N == 2) DMA_ASM(DMA1(0, "") DMA1(1024, ""));
    if constexpr (N == 3) DMA_ASM(DMA1(0, "") DMA1(1024, "") DMA1(2048, ""));
    if constexpr (N == 4) DMA_ASM(DMA1(0, "") DMA1(1024, "") DMA1(2048, "") DMA1(3072, ""));
  }
#undef DMA_ASM
}
__device__ __forceinline__ void dma4v(const unsigned (&v)[4], u32x4 d, unsigned soff, unsigned lds) {
  asm volatile("s_mov_b32 m0, %6\n\ts_nop 0\n\t"
               "buffer_load_dwordx4 %0, %4, %5 offen lds\n\t"
               "buffer_load_dwordx4 %1, %4, %5 offen offset:1024 lds\n\t"
               "buffer_load_dwordx4 %2, %4, %5 offen offset:2048 lds\n\t"
               "buffer_load_dwordx4 %3, %4, %5 offen offset:3072 lds"
               :: "v"(v[0]), "v"(v[1]), "v"(v[2]), "v"(v[3]), "s"(d), "s"(soff), "s"(lds) : "memory");
}

typedef __amdgpu_buffer_rsrc_t rsrc_t;
__device__ __forceinline__ void st8(rsrc_t r, unsigned voff, unsigned soff, u32x2 v) { __builtin_amdgcn_raw_buffer_store_b64(v, r, voff, soff, 16); }
__device__ __forceinline__ void st16(rsrc_t r, unsigned voff, unsigned soff, u32x4 v) { __builtin_amdgcn_raw_buffer_store_b128(v, r, voff + soff, 0, 16); }
__device__ __forceinline__ void st4(rsrc_t r, unsigned voff, unsigned soff, unsigned v) { __builtin_amdgcn_raw_buffer_store_b32(v, r, voff, soff, 16); }
__device__ __forceinline__ u32x4 ld16(rsrc_t r, unsigned voff, unsigned soff) { return __builtin_amdgcn_raw_buffer_load_b128(r, voff, soff, 16); }
__device__ __forceinline__ u32x2 ld8(rsrc_t r, unsigned voff, unsigned soff) { return __builtin_amdgcn_raw_buffer_load_b64(r, voff, soff, 16); }
__device__ __forceinline__ unsigned ld4(rsrc_t r, unsigned voff, unsigned soff) { return __builtin_amdgcn_raw_buffer_load_b32(r, voff, soff, 16); }

__device__ __forceinline__ unsigned read_tag(unsigned epoch_v, float dep) { unsigned t; asm volatile("s_nop 0\n\tv_readfirstlane_b32 %0, %1\n\ts_nop 4" : "=s"(t) : "v"(epoch_v), "v"(dep)); return t + 1u; }
__device__ __forceinline__ u32x2 flagval(unsigned tag) { return u32x2{tag, tag * 0x9E3779B1u + 0x7F4A7C15u}; }
template <unsigned STR = 128u>
__device__ __forceinline__ void poll_flags(rsrc_t r, int fidx, unsigned tag) {
  const u32x2 want = flagval(tag);
  const unsigned off = (unsigned)(fidx < 0 ? 0 : fidx) * STR;
#pragma unroll 1
  for (int spin = 0; spin < (1 << 16); ++spin) {
    const u32x2 v = ld8(r, off, WS_FLAG);
    if (__all(fidx < 0 || (v.x == want.x && v.y == want.y))) break;
    __builtin_amdgcn_s_sleep(1);
  }
  __builtin_amdgcn_fence(__ATOMIC_ACQUIRE, "wavefront");
  asm volatile("" ::: "memory");
}
__device__ __forceinline__ bool arrive_last(lptr L, int cnt, unsigned n) {
  asm volatile("s_waitcnt vmcnt(0) lgkmcnt(0)" ::: "memory");
  unsigned old = 0;
  if ((threadIdx.x & 63) == 0) old = __hip_atomic_fetch_add((LAS unsigned*)(L + L_CNT + 4 * cnt), 1u, __ATOMIC_RELAXED, __HIP_MEMORY_SCOPE_WORKGROUP);
  old = rfl(old);
  asm volatile("" ::: "memory");
  return old == n - 1;
}
__device__ __forceinline__ void group_sync(lptr L, int cnt, unsigned n) {
  asm volatile("s_waitcnt lgkmcnt(0)" ::: "memory");
  if ((threadIdx.x & 63) == 0) __hip_atomic_fetch_add((LAS unsigned*)(L + L_CNT + 4 * cnt), 1u, __ATOMIC_RELAXED, __HIP_MEMORY_SCOPE_WORKGROUP);
#pragma unroll 1
  for (int spin = 0; spin < (1 << 22); ++spin) { if (rfl(*(volatile LAS unsigned*)(L + L_CNT + 4 * cnt)) >= n) break; }
  asm volatile("" ::: "memory");
}
__device__ __forceinline__ void raise_flag(rsrc_t r, unsigned fidx, unsigned tag) { if ((threadIdx.x & 63) == 0) st8(r, 0, WS_FLAG + fidx * 128u, flagval(tag)); }
__device__ __forceinline__ void lds_signal(lptr L, int cnt) { asm volatile("" ::: "memory"); if ((threadIdx.x & 63) == 0) *(volatile LAS unsigned*)(L + L_CNT + 4 * cnt) = 1u; }
__device__ __forceinline__ void lds_wait(lptr L, int cnt) {
#pragma unroll 1
  for (int spin = 0; spin < (1 << 22); ++spin) { if (rfl(*(volatile LAS unsigned*)(L + L_CNT + 4 * cnt))) break; __builtin_amdgcn_s_sleep(2); }
  asm volatile("" ::: "memory");
}

__device__ __forceinline__ u32x4 pair16(u32x2 a, u32x2 b) {
  const auto x = __builtin_amdgcn_permlane16_swap(a.x, b.x, false, false), y = __builtin_amdgcn_permlane16_swap(a.y, b.y, false, false);
  return u32x4{x[0], y[0], x[1], y[1]};
}
__device__ __forceinline__ void retile_read(lptr u, int lane, u32x4 (&f)[4]) {
  const lptr rp = u + (8 * (lane >> 4) + ((lane & 15) >> 2)) * 128 + (lane & 3) * 8;
#pragma unroll
  for (int n = 0; n < 4; ++n) f[n] = __builtin_bit_cast(u32x4, cat8(trread(rp + n * 32), trread(rp + n * 32 + 512)));
}

__host__ __device__ constexpr u64 above_mask(int e) {
  u64 m = 0;
  for (int l = 0; l < 64; ++l) if ((e & 3) + 8 * (e >> 2) + 4 * (l >> 5) > (l & 31)) m |= 1ull << l;
  return m;
}

__global__ __launch_bounds__(512) __attribute__((amdgpu_waves_per_eu(2, 2)))
void mega(const u16* __restrict__ x, const u16* __restrict__ wqkv, const u16* __restrict__ wo, const u16* __restrict__ wg, const u16* __restrict__ wu,
          const u16* __restrict__ wd, u16* __restrict__ out, char* __restrict__ ws) {
  extern __shared__ __attribute__((aligned(16))) char smem[];
  const lptr L = (lptr)smem;
  const unsigned lds0 = (unsigned)(uintptr_t)L;
  const int bid = blockIdx.x, xcd = bid & 7, idx = bid >> 3;
  const int tid = threadIdx.x, lane = tid & 63, wid = rfl(tid >> 6), w4 = wid & 3;
  const bool isC = wid < 4;
  const int r = lane & 15, g = lane >> 4;
  const unsigned vo = lane * 16;
  const rsrc_t rws = __builtin_amdgcn_make_buffer_rsrc(ws, 0, WS_END, 0x00020000);
  const u32x4 dws = mkdesc(ws, WS_END);
  u64 padt = __builtin_amdgcn_s_memrealtime();
  STAMP(0);
  unsigned epoch_v;
  asm volatile("buffer_load_dword %0, off, %1, 0 offset:0 sc1" : "=v"(epoch_v) : "s"(mkdesc(ws + WS_FLAG + F_EPOCH * 128, 128)) : "memory");
  if (tid < 16) *(LAS unsigned*)(L + L_CNT + 4 * tid) = 0u;
  unsigned tag;

  const int tm = 4 * (xcd >> 1) + (idx & 3), tn = (8 * (xcd & 1) + (idx >> 2)) ^ (idx >= 30 ? 8 : 0);
  LAS f32x2* st_tok = (LAS f32x2*)(L + L_MISC);
  LAS float* st_col = (LAS float*)(L + L_MISC + 1024);
  const int wgid = tm * 16 + tn;
  auto row_stats = [&]() {
    const int row = 128 * tm + 8 * tn + 2 * w4 + (lane >> 5);
    u32x2 xv[5];
#pragma unroll
    for (int i = 0; i < 5; ++i) xv[i] = *(const u32x2*)(x + row * D_ + ((lane & 31) + 32 * i) * 4);
    float s1 = 0.f, s2 = 0.f;
#pragma unroll
    for (int i = 0; i < 5; ++i) s1 += (bflo(xv[i].x) + bfhi(xv[i].x)) + (bflo(xv[i].y) + bfhi(xv[i].y));
    const float mean = sum32(s1) * (1.0f / D_);
#pragma unroll
    for (int i = 0; i < 5; ++i) { const float a = bflo(xv[i].x) - mean, b = bfhi(xv[i].x) - mean, c = bflo(xv[i].y) - mean, d = bfhi(xv[i].y) - mean; s2 += (a * a + b * b) + (c * c + d * d); }
    const float sd = bfr(__builtin_sqrtf(bfr(bfr(sum32(s2) * (1.0f / D_)) + LN_EPS)));
    const unsigned t = read_tag(epoch_v, sd), gt = flagval(t).y;
    if ((lane & 31) == 0) st16(rws, row * 16, WS_S1, u32x4{__float_as_uint(bfr(mean)), gt, __float_as_uint(1.0f / sd), gt});
    return t;
  };
  u32x4 rtf[2][4];
  unsigned rtd[2][4];
#pragma unroll
  for (int i = 0; i < 4; ++i) { rtf[0][i] = u32x4{0u, 0u, 0u, 0u}; rtf[1][i] = rtf[0][i]; rtd[0][i] = 0u; rtd[1][i] = 0u; }
  unsigned csd = 0u;
  if (tn < 15) {
    const int aid = tm * 15 + tn;
    const bool gu_ok = aid < 216, d_ok = aid < 180;
    const int cb = (gu_ok ? aid : 0) >> 2, isup = cb >= 27, cbb = cb - 27 * isup;
    const int sA = 5 * (aid & 3) + w4, sB = 5 * (aid & 3) + 4;
    const int ud = (d_ok ? aid : 0) * 3 + (w4 > 0 ? w4 - 1 : 0), dcb = ud / 54, dsx = ud - 54 * dcb;
    if (!isC) {
      __builtin_amdgcn_s_setprio(LPRIO);
      unsigned voffX[4], voffW[4];
#pragma unroll
      for (int i = 0; i < 4; ++i) {
        const int p = w4 * 4 + i;
        voffX[i] = (unsigned)((8 * p + (lane >> 3)) * 1280 + (((lane & 7) ^ ((lane >> 4) | (4 * (p & 1)))) * 16) - 1024 * i);
        voffW[i] = (unsigned)((4 * p + (lane >> 4)) * 3840 + ((((((lane & 15) >> 2) ^ (lane >> 4)) << 2) | (lane & 3)) * 16) - 1024 * i);
      }
      const u32x4 dx = mkdesc(x, 2048 * 1280), dw = mkdesc(wqkv, 640 * 3840);
      const unsigned xs0 = tm * 128 * 1280, ws0 = tn * 256, ld0 = lds0 + w4 * 4096;
      const int ks0 = tm < 10 ? tm : tm - 10;
      int ks = ks0, slot = 0;
#define P1_ISSUE() do { dma4v(voffX, dx, xs0 + ks * 128, ld0 + slot * P1_STAGE); dma4v(voffW, dw, ws0 + ks * (64 * 3840), ld0 + slot * P1_STAGE + 16384); \
        ks = ks == 9 ? 0 : ks + 1; slot = slot == 2 ? 0 : slot + 1; } while (0)
      const int tl = lane < 12 ? lane : 0;
      const char* tp = tl < 4 ? (const char*)x + (128 * tm + 16 * (idx >> 2) + 4 * w4 + tl) * 1280 : (const char*)wqkv + (16 * (idx & 3) + 4 * w4 + ((tl - 4) >> 1)) * 3840 + tn * 256 + (tl & 1) * 128;
      const int tstep = tl < 4 ? 128 : 64 * 3840;
      unsigned junk = 0;
#define T_ISSUE(st) do { if (TOUCH) asm volatile("global_load_dword %0, %1, off" : "+v"(junk) : "v"(tp + ((st) + ks0 >= 10 ? (st) + ks0 - 10 : (st) + ks0) * tstep) : "memory"); } while (0)
      constexpr int T1 = TOUCH ? 1 : 0;
      P1_ISSUE(); T_ISSUE(2);
      P1_ISSUE(); T_ISSUE(3); waitv_bar<8 + 2 * T1>();
      tag = read_tag(epoch_v, 0.f);
      const unsigned gt = flagval(tag).y;
      P1_ISSUE(); T_ISSUE(4);
      if (tm < 10) {
        const bf16x8 ones = {0x3F80, 0x3F80, 0x3F80, 0x3F80, 0x3F80, 0x3F80, 0x3F80, 0x3F80};
        const lptr cp = L + 16384 + (8 * g + (r >> 2)) * 256 + ((w4 ^ (r >> 2)) * 64) + (lane & 3) * 8;
        f32x4 c[2];
#pragma unroll
        for (int hf = 0; hf < 2; ++hf) {
          c[hf] = MFMA16(cat8(trread(cp + hf * 32), trread(cp + hf * 32 + 1024)), ones, (f32x4{0.f, 0.f, 0.f, 0.f}));
          c[hf] = MFMA16(cat8(trread(cp + hf * 32 + 8192), trread(cp + hf * 32 + 9216)), ones, c[hf]);
        }
        const f32x4 cc = (r & 2) ? c[1] : c[0];
        if (r < 4) st16(rws, (r >> 1) * 128 + g * 32 + (r & 1) * 16, WS_C1 + (tn * 10 + tm) * 1024 + w4 * 256,
                        u32x4{__float_as_uint((r & 1) ? cc[2] : cc[0]), gt, __float_as_uint((r & 1) ? cc[3] : cc[1]), gt});
      }
      waitv_bar<8 + 2 * T1>();
#pragma unroll 1
      for (int st = 3; st < 8; ++st) { P1_ISSUE(); T_ISSUE(st + 2); waitv_bar<8 + 2 * T1>(); }
      u32x4 gs, gc0, gc1, gc2;
      const unsigned gsv = (128 * tm + 32 * w4 + (lane & 31)) * 16 + WS_S1, gcv = WS_C1 + tn * 10240 + g * 1024 + w4 * 256 + r * 16, gcw = gcv + (g < 2 ? 8192 : 0);
      P1_ISSUE();
      asm volatile("buffer_load_dwordx4 %0, %4, %7, 0 offen sc1\n\tbuffer_load_dwordx4 %1, %5, %7, 0 offen sc1\n\tbuffer_load_dwordx4 %2, %5, %7, %8 offen sc1\n\tbuffer_load_dwordx4 %3, %6, %7, 0 offen sc1"
                   : "=&v"(gs), "=&v"(gc0), "=&v"(gc1), "=&v"(gc2) : "v"(gsv), "v"(gcv), "v"(gcw), "s"(dws), "s"(4096u) : "memory");
      waitv_bar<12 + T1>();
      P1_ISSUE();
      waitv_bar<12>();
      asm volatile("s_waitcnt vmcnt(0) lgkmcnt(0)\n\ts_barrier" : "+v"(gs), "+v"(gc0), "+v"(gc1), "+v"(gc2), "+v"(junk) :: "memory");
#pragma unroll 1
      for (int spin = 0; spin < (1 << 16); ++spin) {
        if (__all(gs.y == gt && gs.w == gt && gc0.y == gt && gc0.w == gt && gc1.y == gt && gc1.w == gt && gc2.y == gt && gc2.w == gt)) break;
        gs = ld16(rws, gsv, 0); gc0 = ld16(rws, gcv, 0); gc1 = ld16(rws, gcv, 4096); gc2 = ld16(rws, gcw, 0);
      }
      if (lane < 32) st_tok[32 * w4 + lane] = f32x2{__uint_as_float(gs.x), __uint_as_float(gs.z)};
      { const float ca = add32(add16(__uint_as_float(gc0.x) + __uint_as_float(gc1.x) + (g < 2 ? __uint_as_float(gc2.x) : 0.f)));
        const float cb = add32(add16(__uint_as_float(gc0.z) + __uint_as_float(gc1.z) + (g < 2 ? __uint_as_float(gc2.z) : 0.f)));
        if (lane < 16) *(LAS f32x2*)(st_col + w4 * 32 + lane * 2) = f32x2{ca, cb}; }
      bar_l();
      lds_wait(L, 12);
      const u32x4 dgu = mkdesc(isup ? wu : wg, 640 * 3456), dB = w4 == 0 ? dgu : mkdesc(wd, 1728 * 1280);
      const unsigned ldB = w4 == 0 ? 3456 : 1280;
      unsigned vA[4], vB[4];
#pragma unroll
      for (int i = 0; i < 4; ++i) { vA[i] = (8 * i + (lane >> 3)) * 3456 + (lane & 7) * 16 - 1024 * i; vB[i] = (8 * i + (lane >> 3)) * ldB + (lane & 7) * 16 - 1024 * i; }
      dma4v(vA, dgu, sA * (32 * 3456) + cbb * 128, lds0 + L_RT + w4 * 4096);
      dma4v(vB, dB, w4 == 0 ? sB * (32 * 3456) + cbb * 128 : dsx * (32 * 1280) + dcb * 128, lds0 + L_RT + 16384 + w4 * 4096);
#pragma unroll
      for (int n = 0; n < 4; ++n) {
        const int t = 4 * dcb + n, sl = t / 5;
        rtd[0][n] = gu_ok ? WS_WGU + ((sA * 108 + 4 * cbb + n) * 2 + isup) * 1024 : 0u;
        rtd[1][n] = w4 == 0 ? (gu_ok ? WS_WGU + ((sB * 108 + 4 * cbb + n) * 2 + isup) * 1024 : 0u) : (d_ok ? WS_WD + ((sl * 54 + dsx) * 5 + (t - 5 * sl)) * 1024 : 0u);
      }
      if (w4 == 1 && gu_ok) csd = WS_CS + ((aid & 3) * 3456 + cb * 64) * 4;
      waitv<0>();
      retile_read(L + L_RT + w4 * 4096, lane, rtf[0]);
      retile_read(L + L_RT + 16384 + w4 * 4096, lane, rtf[1]);
      { const bf16x8 ones = {0x3F80, 0x3F80, 0x3F80, 0x3F80, 0x3F80, 0x3F80, 0x3F80, 0x3F80};
#pragma unroll
        for (int n = 0; n < 4; ++n) {
          f32x4 c = MFMA16(__builtin_bit_cast(bf16x8, rtf[0][n]), ones, (f32x4{0.f, 0.f, 0.f, 0.f}));
          if (w4 == 0) c = MFMA16(__builtin_bit_cast(bf16x8, rtf[1][n]), ones, c);
          if (r == 0) *(LAS f32x4*)(L + L_GUCS + w4 * 256 + n * 64 + g * 16) = c;
        } }
    } else {
      const int wr = w4 >> 1, wc = w4 & 1;
      tag = row_stats();
      const int t32 = lane & 31, hh = lane >> 5;
      const int xo = (64 * wr + t32) * 128 + ((hh ^ ((t32 >> 1) & 7)) * 16);
      int wo_[2];
#pragma unroll
      for (int n = 0; n < 2; ++n) wo_[n] = 16384 + (8 * hh + (r >> 2)) * 256 + (((2 * wc + n) ^ (r >> 2)) * 64) + (g & 1) * 32 + (lane & 3) * 8;
      f32x16 acc[2][2];
#pragma unroll
      for (int n = 0; n < 2; ++n)
#pragma unroll
        for (int m = 0; m < 2; ++m)
#pragma unroll
          for (int e = 0; e < 16; ++e) acc[n][m][e] = 0.f;
      bf16x8 xa[2][2], wa[2][2], xb[2][2], wb[2][2];
#define P1_READ(xf, wf, B, kk) do { _Pragma("unroll") for (int k = 0; k < 2; ++k) { \
        xf[k][0] = lds16(B + (xo ^ (64 * kk + 32 * k))); \
        wf[k][0] = cat8(trread(B + wo_[0] + 8192 * kk + 4096 * k), trread(B + wo_[0] + 8192 * kk + 4096 * k + 1024)); \
        xf[k][1] = lds16(B + (xo ^ (64 * kk + 32 * k)) + 4096); \
        wf[k][1] = cat8(trread(B + wo_[1] + 8192 * kk + 4096 * k), trread(B + wo_[1] + 8192 * kk + 4096 * k + 1024)); } } while (0)
#define P1_MMA(xf, wf) do { _Pragma("unroll") for (int k = 0; k < 2; ++k) _Pragma("unroll") for (int n = 0; n < 2; ++n) _Pragma("unroll") for (int m = 0; m < 2; ++m) \
        acc[n][m] = MFMA32(wf[k][n], xf[k][m], acc[n][m]); \
        _Pragma("unroll") for (int i = 0; i < 6; ++i) { SGB(0x008, 1); SGB(0x100, 2); } SGB(0x008, 2); } while (0)
      unsigned junk = 0;
      { const char* gp = (const char*)(isup ? wu : wg) + cbb * 128;
        const char* tp = (lane < 32 || w4 == 0) ? gp + ((lane < 32 ? sA : sB) * 32 + (lane & 31)) * 3456 : (const char*)wd + (dsx * 32 + (lane & 31)) * 1280 + dcb * 128;
        bar_l();
        if (RTOUCH) asm volatile("global_load_dword %0, %1, off" : "+v"(junk) : "v"(tp) : "memory"); }
      STAMP(17);
      P1_READ(xa, wa, L, 0);
      SB();
      int slot = 0;
#pragma unroll 1
      for (int st = 0; st < 10; ++st) {
        const lptr B = L + slot * P1_STAGE;
        slot = slot == 2 ? 0 : slot + 1;
        const lptr Bn = L + slot * P1_STAGE;
        P1_READ(xb, wb, B, 1);
        P1_MMA(xa, wa);
        bar_l();
        P1_READ(xa, wa, Bn, 0);
        P1_MMA(xb, wb);
        SB();
      }
      STAMP(1);
      const int tensor = tn / 5;
      const float osc = tensor == 0 ? QSCALE : 1.0f;
      f32x2 ts[2];
#pragma unroll
      for (int m = 0; m < 2; ++m) { ts[m] = st_tok[64 * wr + 32 * m + t32]; ts[m].y *= osc; }
      const unsigned so = WS_QKV + (tensor * 32 * 64 + 4 * tm + 2 * wr) * 1280, dv0 = (g & 1) * 1280 + (r >> 2) * 320 + (r & 3) * 16;
#pragma unroll
      for (int n = 0; n < 2; ++n)
#pragma unroll
        for (int i = 0; i < 4; ++i) {
          const f32x4 cs = *(LAS f32x4*)(st_col + 64 * wc + 32 * n + 8 * i + 4 * hh);
          const int c4 = (tn - 5 * tensor) * 32 + 16 * wc + 8 * n + 2 * i + hh;
          const int head = (c4 * 205) >> 10, dg = c4 - 5 * head;
          const f32x4 v = (f32x4{acc[n][0][4 * i], acc[n][0][4 * i + 1], acc[n][0][4 * i + 2], acc[n][0][4 * i + 3]} - ts[0].x * cs) * ts[0].y;
          const f32x4 w = (f32x4{acc[n][1][4 * i], acc[n][1][4 * i + 1], acc[n][1][4 * i + 2], acc[n][1][4 * i + 3]} - ts[1].x * cs) * ts[1].y;
          st16(rws, dv0 + head * (64 * 1280) + dg * 64, so, pair16(u32x2{cvtpk(v[0], v[1]), cvtpk(v[2], v[3])}, u32x2{cvtpk(w[0], w[1]), cvtpk(w[2], w[3])}));
        }
      STAMP(2);
      if (w4 == 3) lds_signal(L, 12);
      asm volatile("s_waitcnt vmcnt(0)" : "+v"(junk) :: "memory");
      if (arrive_last(L, 0, 4)) raise_flag(rws, F_Q + wgid, tag);
    }
  } else {
    if (isC) (void)row_stats();
    else if (WARM) {
      unsigned acc = 0;
      if (w4 == 0) {
        const char* pc; int len;
        asm volatile("s_getpc_b64 %0\n.Lmega_here:\n\ts_mov_b32 %1, .Lmega_tail-.Lmega_here" : "=s"(pc), "=s"(len));
#pragma unroll
        for (int i = 0; i < 5; ++i) { const int o = (i * 64 + lane) * 128; acc += *(const volatile unsigned*)(pc + (o < len ? o : 0)); }
      } else if (w4 == 1) {
        const int o = (lane < 34 ? lane : 33) * 32768;
        acc += *(const volatile u16*)(wg + o) + *(const volatile u16*)(wu + o) + *(const volatile u16*)(wd + o) + *(const volatile u16*)(out + (lane < 40 ? lane : 39) * 32768);
      } else {
#pragma unroll
        for (int i = 0; i < 4; ++i) acc += ld4(rws, ((w4 - 2) * 256 + i * 64 + lane) * 65536u, 0);
      }
      asm volatile("" :: "v"(acc));
    }
    const u32x4 dwo = mkdesc(wo, 640 * 1280);
    const int u0 = tm * 8 + wid;
    unsigned wod[2][4];
#pragma unroll
    for (int k = 0; k < 2; ++k) {
      const int u = (k == 1 && u0 + 128 < 200) ? u0 + 128 : u0, s0 = u / 10, c0 = u - 10 * s0, j = lane >> 3;
      unsigned vd[4];
#pragma unroll
      for (int i = 0; i < 4; ++i) {
        const int ch = 4 * s0 + i;
        const int row = ch < 64 ? 20 * (ch >> 1) + 4 * (ch & 1) + (j & 3) + 8 * (j >> 2) : 20 * (2 * (ch - 64) + (j >> 2)) + 16 + (j & 3);
        vd[i] = row * 1280 + (lane & 7) * 16 - 1024 * i;
        const int t = 4 * c0 + i, sl = t / 5;
        wod[k][i] = WS_WO + ((sl * 20 + s0) * 5 + (t - 5 * sl)) * 1024;
      }
      dma4v(vd, dwo, c0 * 128, lds0 + L_RT - k * 32768 + wid * 4096);
    }
    waitv_bar<0>();
    tag = read_tag(epoch_v, 0.f);
#pragma unroll
    for (int k = 0; k < 2; ++k) {
      u32x4 f[4];
      retile_read(L + L_RT - k * 32768 + wid * 4096, lane, f);
#pragma unroll
      for (int n = 0; n < 4; ++n) st16(rws, vo, wod[k][n], f[n]);
    }
    if (arrive_last(L, 5, 8)) raise_flag(rws, F_Q + wgid, tag);
  }
  STAMP(3);

  const int head = 4 * xcd + (idx >> 3), qb = idx & 7;
  const int rt = 4 * xcd + (idx & 3), ct = idx >> 2;
  if (isC) {
    if (wid == 0) {
      int f = -1;
      if (lane < 24) {
        const int tmi = lane / 6, wh = lane - 6 * tmi, tmm = 2 * qb - 2 + tmi, ten = wh >> 1;
        const int tnn = 5 * ten + ((20 * head + ((wh & 1) ? 19 : 0)) >> 7);
        if (tmm >= 0 && (ten > 0 || tmi >= 2)) f = F_Q + tmm * 16 + tnn;
      } else if (lane < 40) f = F_Q + (lane - 24) * 16 + 15;
      poll_flags(rws, f, tag);
      lds_signal(L, 8);
    } else lds_wait(L, 8);
    STAMP(4);
    const unsigned hb = WS_QKV + (head * 64 + 8 * qb) * 1280;
    const unsigned kv = hb + (32 * 64 * 1280) + (w4 & 1) * 5120 - ((w4 < 2 && qb > 0) ? 10240 : 0);
    if (w4 < 2) dma_run<3, true>(vo, dws, hb + w4 * 3072, lds0 + A_Q + w4 * 3072);
    else dma_run<2, true>(vo, dws, hb + 6144 + (w4 - 2) * 2048, lds0 + A_Q + 6144 + (w4 - 2) * 2048);
    dma_run<4, true>(vo, dws, kv, lds0 + A_K + w4 * 5120); dma_run<1, true>(vo, dws, kv + 4096, lds0 + A_K + w4 * 5120 + 4096);
    dma_run<4, true>(vo, dws, kv + 32 * 64 * 1280, lds0 + A_V + w4 * 5120); dma_run<1, true>(vo, dws, kv + 32 * 64 * 1280 + 4096, lds0 + A_V + w4 * 5120 + 4096);
  } else if (wid == 4) *(LAS u32x4*)(L + A_ONE + vo) = u32x4{0x00003F80u, 0u, 0x00003F80u, 0u};
  __builtin_amdgcn_s_setprio(0);
  u32x4 ma, mb;
#pragma unroll
  for (int e = 0; e < 4; ++e) {
    const int ka = 2 * (e & 1) + 8 * (e >> 1) + 4 * (lane >> 5), kb = ka + 16, c = lane & 31;
    ma[e] = (ka > c ? 0xffffu : 0u) | (ka + 1 > c ? 0xffff0000u : 0u);
    mb[e] = (kb > c ? 0xffffu : 0u) | (kb + 1 > c ? 0xffff0000u : 0u);
  }
  asm volatile("" : "+v"(ma), "+v"(mb));
  waitv_bar<5>();
  STAMP(5);
  if (!isC) {
#pragma unroll
    for (int i = 0; i < 8; ++i) if (rtd[i >> 2][i & 3]) st16(rws, vo, rtd[i >> 2][i & 3], rtf[i >> 2][i & 3]);
  }
  if (csd) { const LAS float* cp = (const LAS float*)(L + L_GUCS) + lane; st4(rws, lane * 4, csd, __float_as_uint((cp[0] + cp[64]) + (cp[128] + cp[192]))); }
  {
    const int c = lane & 31, hh = lane >> 5;
    const int nskip = qb == 0 ? 8 - wid : 0;
    const unsigned wos = WS_WO + ct * 102400 + wid * 12288, wod = lds0 + L_WO + wid * 12288;
#define WO_PIECE(i) dma_run<1, true>(vo, dws, wos + (i) * 1024, wod + (i) * 1024)
    bf16x8 q1; u32x2 q2;
    const int ko = wid * 1280 + ((c >> 2) & 3) * 320 + (c & 3) * 16 + (c >> 4) * 8;
#define LD8(p) (*(volatile LAS s16x4*)(p))
    { const lptr qp = L + A_Q + ko;
      q1 = cat8(LD8(qp + 128 * hh), LD8(qp + 128 * hh + 64));
      q2 = __builtin_bit_cast(u32x2, LD8(qp + 256)); if (hh) q2 = u32x2{0x3F80u, 0u}; }
    const lptr kp = L + A_K + ko;
    f32x16 s[9];
    const f32x16 zero16 = {0.f,0.f,0.f,0.f,0.f,0.f,0.f,0.f,0.f,0.f,0.f,0.f,0.f,0.f,0.f,0.f};
    bf16x8 k1[9]; u32x2 k2[9];
#pragma unroll
    for (int jj = 0; jj < 9; ++jj) {
      const int jb = jj == 0 ? 0 : jj == 1 ? 8 : jj - 1;
      k1[jb] = cat8(LD8(kp + jb * 1280 + 128 * hh), LD8(kp + jb * 1280 + 128 * hh + 64));
      k2[jb] = __builtin_bit_cast(u32x2, LD8(kp + jb * 1280 + 256));
    }
    SB();
    float m0, m1;
#pragma unroll
    for (int jj = 0; jj < 11; ++jj) {
      if (jj < 9) {
        const int jb = jj == 0 ? 0 : jj == 1 ? 8 : jj - 1;
        k2[jb].x = hh ? (jb < nskip ? 0xFF7Fu : 0u) : k2[jb].x;
        s[jb] = MFMA32(k1[jb], q1, zero16);
        s[jb] = MFMA32K8(__builtin_bit_cast(s16x4, k2[jb]), __builtin_bit_cast(s16x4, q2), s[jb]);
      }
      if (jj == 3) {
#pragma unroll
        for (int e = 0; e < 16; ++e) s[0][e] = __builtin_amdgcn_inverse_ballot_w64(above_mask(e)) ? s[0][e] : s[8][e];
        m0 = fmaxf(s[0][0], s[0][1]); m1 = fmaxf(s[0][2], s[0][3]);
#pragma unroll
        for (int e = 4; e < 16; e += 4) { m0 = fmaxf(fmaxf(m0, s[0][e]), s[0][e + 1]); m1 = fmaxf(fmaxf(m1, s[0][e + 2]), s[0][e + 3]); }
      }
      if (jj >= 4) {
        const int jb = jj - 3;
#pragma unroll
        for (int e = 0; e < 16; e += 4) { m0 = fmaxf(fmaxf(m0, s[jb][e]), s[jb][e + 1]); m1 = fmaxf(fmaxf(m1, s[jb][e + 2]), s[jb][e + 3]); }
      }
      if (jj & 1) WO_PIECE(jj >> 1);
      SB();
    }
    float mx = fmaxf(m0, m1);
    { auto rr = __builtin_amdgcn_permlane32_swap(__float_as_uint(mx), __float_as_uint(mx), false, false);
      mx = fmaxf(__uint_as_float(rr[0]), __uint_as_float(rr[1])); }
    STAMP(28);
    if (isC) { waitv<5>(); if (lane == 0) __hip_atomic_fetch_add((LAS unsigned*)(L + L_CNT + 4 * 7), 1u, __ATOMIC_RELAXED, __HIP_MEMORY_SCOPE_WORKGROUP); }
    u32x2 nm;
    { const unsigned hi = __float_as_uint(-mx) & 0xffff0000u; const float r1 = -mx - __uint_as_float(hi);
      const unsigned mid = __float_as_uint(r1) & 0xffff0000u, lo = __float_as_uint(r1 - __uint_as_float(mid));
      nm = hh ? u32x2{0u, 0u} : u32x2{(hi >> 16) | mid, lo >> 16}; }
    const s16x4 one4 = {0x3F80, 0x3F80, 0x3F80, 0x3F80};
#define S_SHIFT(jb) s[jb] = MFMA32K8(one4, __builtin_bit_cast(s16x4, nm), s[jb])
    S_SHIFT(1);
#pragma unroll 1
    for (int spin = 0; spin < (1 << 22); ++spin) { if (rfl(*(volatile LAS unsigned*)(L + L_CNT + 4 * 7)) >= 4u) break; }
    asm volatile("" ::: "memory");
    SB();
    STAMP(29);
    const int gq = r >> 2, gp = lane & 3, up16 = (lane >> 4) & 1;
    const bool vconst = up16 && gp > 0;
    const lptr vb0 = L + (vconst ? A_ONE + 8 + gq * 16 : A_V + wid * 1280 + hh * 320 + (4 * up16 + gp) * 64 + gq * 16);
    const int vstep = vconst ? 0 : 1280;
    f32x16 o = zero16;
    bf16x8 va[2], vb[2], vc, vd;
#define V_READ(a, b, jb) do { const lptr v0 = vb0 + (jb) * vstep; a = cat8(trread(v0), trread(v0 + 640)); b = cat8(trread(v0 + 8), trread(v0 + 648)); } while (0)
    u32x4 pa[2], pb[2];
#pragma unroll
    for (int it = 0; it < 9; ++it) {
      const int jb = it == 7 ? 0 : it + 1;
      if (it < 8) {
        V_READ(va[it & 1], vb[it & 1], jb);
        if (it == 7) V_READ(vc, vd, 8);
        if (it < 7) S_SHIFT(it == 6 ? 0 : it + 2);
        float p[16];
#pragma unroll
        for (int e = 0; e < 16; ++e) p[e] = __builtin_amdgcn_exp2f(s[jb][e]);
        pa[it & 1] = u32x4{cvtpk(p[0], p[1]), cvtpk(p[2], p[3]), cvtpk(p[4], p[5]), cvtpk(p[6], p[7])};
        pb[it & 1] = u32x4{cvtpk(p[8], p[9]), cvtpk(p[10], p[11]), cvtpk(p[12], p[13]), cvtpk(p[14], p[15])};
      }
      if (it >= 1 && it < 8) {
        o = MFMA32(va[(it - 1) & 1], __builtin_bit_cast(bf16x8, pa[(it - 1) & 1]), o);
        o = MFMA32(vb[(it - 1) & 1], __builtin_bit_cast(bf16x8, pb[(it - 1) & 1]), o);
        SGB(0x100, 4); SGB(0x008, 1); SGB(0x402, 5); SGB(0x008, 1); SGB(0x402, 5); SGB(0x008, 1);
      }
      if (it == 8) {
        o = MFMA32(va[1], __builtin_bit_cast(bf16x8, pa[1] & ma), o);
        o = MFMA32(vb[1], __builtin_bit_cast(bf16x8, pb[1] & mb), o);
        o = MFMA32(vc, __builtin_bit_cast(bf16x8, pa[1] & ~ma), o);
        o = MFMA32(vd, __builtin_bit_cast(bf16x8, pb[1] & ~mb), o);
      }
      if (it < 7) WO_PIECE(5 + it);
      if (it == 7) dma_run<1, true>(vo, dws, WS_WO + ct * 102400 + 98304 + w4 * 1024, lds0 + L_WO + 98304 + w4 * 1024);
      SB();
    }
    const float rl = __builtin_amdgcn_rcpf(hh ? o[8] : o[12]);
    const unsigned ob = WS_O + (16 * qb + 2 * wid) * 20480, ov = (c >> 4) * 20480 + (c & 15) * 16;
    st16(rws, ov + hh * 256, ob + head * 512, u32x4{cvtpk(o[0] * rl, o[1] * rl), cvtpk(o[2] * rl, o[3] * rl), cvtpk(o[4] * rl, o[5] * rl), cvtpk(o[6] * rl, o[7] * rl)});
    if (hh == 0) st8(rws, ov, ob + 16384 + (head >> 1) * 256 + (head & 1) * 8, u32x2{cvtpk(o[8] * rl, o[9] * rl), cvtpk(o[10] * rl, o[11] * rl)});
    STAMP(6);
  }
  waitv_bar<0>();
  if (wid == 0) raise_flag(rws, F_O + qb * 32 + head, tag);
  if (wid == 1) raise_flag(rws, F_RT + (tn < 15 ? tm * 15 + tn : 240 + tm), tag);
  STAMP(7);

  const int pn = 4 * (xcd >> 1) + (idx & 3), cgp = 8 * (xcd & 1) + (idx >> 2);
  const int t0 = cgp < 12 ? 7 * cgp : 6 * cgp + 12, ntile = cgp < 12 ? 7 : 6;
  const int T5 = 4 * rt + w4;
  u32x2 hreg[5];
  const unsigned wgs = WS_WGU + t0 * 2048 + (w4 >> 1) * (108 * 2048) + (w4 & 1) * 7168;
  const int rot4 = P4_ROT;
#define KS4(st) ((st) + rot4 >= 10 ? (st) + rot4 - 10 : (st) + rot4)
  if (isC) {
    u32x2 xv[5];
#pragma unroll
    for (int n = 0; n < 5; ++n) xv[n] = *(const u32x2*)(x + (size_t)(16 * T5 + r) * D_ + 80 * ct + 16 * n + 4 * g);
    if (wid == 0) { poll_flags(rws, lane < 32 ? (int)(F_O + (rt >> 2) * 32 + lane) : -1, tag); lds_signal(L, 9); }
    else lds_wait(L, 9);
    STAMP(8);
    u32x4 af[20];
    const int rot3 = (ROT & 2) ? (ct * 5) >> 3 : 0;
#pragma unroll
    for (int s = 0; s < 20; ++s) { int sg = (s >> 2) + rot3; sg = sg >= 5 ? sg - 5 : sg; af[s] = ld16(rws, vo + (s & 3) * 1024, WS_O + T5 * 20480 + sg * 4096); }
    SB();
    f32x4 acc[5];
#pragma unroll
    for (int n = 0; n < 5; ++n) acc[n] = f32x4{0.f, 0.f, 0.f, 0.f};
    bf16x8 wf[2][5];
    lptr B = L + L_WO + vo + rot3 * 20480;
#pragma unroll
    for (int n = 0; n < 5; ++n) wf[0][n] = lds16(B + n * 1024);
    SB();
#pragma unroll
    for (int s = 0; s < 20; ++s) {
      if (s + 1 < 20) {
        if (((s + 1) & 3) == 0) { int sg = ((s + 1) >> 2) + rot3; sg = sg >= 5 ? sg - 5 : sg; B = L + L_WO + vo + sg * 20480; }
#pragma unroll
        for (int n = 0; n < 5; ++n) wf[(s + 1) & 1][n] = lds16(B + (((s + 1) & 3) * 5 + n) * 1024);
      }
#pragma unroll
      for (int n = 0; n < 5; ++n) acc[n] = MFMA16(wf[s & 1][n], __builtin_bit_cast(bf16x8, af[s]), acc[n]);
      SB();
    }
    bar_l();
    STAMP(9);
    float s1 = 0.f, s2 = 0.f;
#pragma unroll
    for (int n = 0; n < 5; ++n)
      hreg[n] = u32x2{cvtpk(acc[n][0] + bflo(xv[n].x), acc[n][1] + bfhi(xv[n].x)), cvtpk(acc[n][2] + bflo(xv[n].y), acc[n][3] + bfhi(xv[n].y))};
    { const unsigned hv = (g & 1) * 512 + (g >> 1) * 256 + r * 16, hsb = WS_H + T5 * 20480 + ct * 2560;
      st16(rws, hv, hsb, pair16(hreg[0], hreg[1]));
      st16(rws, hv, hsb + 1024, pair16(hreg[2], hreg[3]));
      const u32x4 last = pair16(hreg[4], hreg[4]);
      if (!(g & 1)) st16(rws, hv, hsb + 2048, last); }
#pragma unroll
    for (int n = 0; n < 5; ++n) {
      const float a = bflo(hreg[n].x), b = bfhi(hreg[n].x), c = bflo(hreg[n].y), d = bfhi(hreg[n].y);
      s1 += (a + b) + (c + d); s2 += (a * a + b * b) + (c * c + d * d);
    }
    s1 = add32(add16(s1)); s2 = add32(add16(s2));
    if (g == 0) st8(rws, r * 8, WS_HST + (T5 * 8 + ct) * 128, u32x2{__float_as_uint(s1), __float_as_uint(s2)});
    if (arrive_last(L, 3, 4)) raise_flag(rws, F_H + rt * 8 + ct, tag);
  } else {
    __builtin_amdgcn_s_setprio(LPRIO);
    int f = -1;
    if (lane < 24) { const int b = lane >> 2, isup = b >= 3, cb = (t0 >> 2) + b - 3 * isup; if (cb <= ((t0 + ntile - 1) >> 2)) f = F_RT + (cb + 27 * isup) * 4 + (lane & 3); }
    else if (lane < 60) { const int b = (lane - 24) / 18; f = F_RT + (((5 * ct) >> 2) + b) * 18 + (lane - 24 - 18 * b); }
    if (w4 == 0) { poll_flags(rws, f, tag); lds_signal(L, 6); } else lds_wait(L, 6);
#pragma unroll
    for (int st = 0; st < 2; ++st) {
      dma_run<4, true>(vo, dws, wgs + KS4(st) * (2 * 108 * 2048), lds0 + st * P4_W + w4 * 7168);
      dma_run<3, true>(vo, dws, wgs + KS4(st) * (2 * 108 * 2048) + 4096, lds0 + st * P4_W + w4 * 7168 + 4096);
    }
    const int cc = w4 * 64 + lane;
    if (cc < 224) {
      const unsigned o = (cc >= 112 ? 1728 - 112 : 0) + cc + t0 * 16;
      const float t = (__uint_as_float(ld4(rws, o * 4, WS_CS)) + __uint_as_float(ld4(rws, o * 4, WS_CS + 3456 * 4))) +
                      (__uint_as_float(ld4(rws, o * 4, WS_CS + 2 * 3456 * 4)) + __uint_as_float(ld4(rws, o * 4, WS_CS + 3 * 3456 * 4)));
      *(LAS float*)(L + L_MISC + cc * 4) = t;
    }
#pragma unroll
    for (int n = 0; n < 5; ++n) hreg[n] = u32x2{0u, 0u};
    bar_l();
  }
  STAMP(10);

  const int T4 = 8 * pn + 2 * w4;
  const int rot5 = (ROT & 8) ? ct : 0;
#define KS5(st) ((st) + rot5 >= 9 ? (st) + rot5 - 9 : (st) + rot5)
  if (!isC) {
    poll_flags(rws, lane < 8 ? (int)(F_H + (2 * pn + (w4 >> 1)) * 8 + lane) : -1, tag);
    STAMP(11);
    const unsigned as = WS_H + T4 * 20480, ad = lds0 + P4_A0 + w4 * 4096;
#pragma unroll
    for (int st = 0; st < 2; ++st) { dma_run<2, true>(vo, dws, as + KS4(st) * 2048, ad + st * P4_A); dma_run<2, true>(vo, dws, as + 20480 + KS4(st) * 2048, ad + st * P4_A + 2048); }
    u32x2 hs[2][2];
#pragma unroll
    for (int m = 0; m < 2; ++m) { hs[m][0] = ld8(rws, lane * 8, WS_HST + (T4 + m) * 1024); hs[m][1] = ld8(rws, lane * 8, WS_HST + (T4 + m) * 1024 + 512); }
    waitv_bar<8>();
    int slot = 2;
#pragma unroll 1
    for (int st = 2; st < 10; ++st) {
      const int ks = KS4(st);
      dma_run<2, true>(vo, dws, as + ks * 2048, ad + slot * P4_A); dma_run<2, true>(vo, dws, as + 20480 + ks * 2048, ad + slot * P4_A + 2048);
      dma_run<4, true>(vo, dws, wgs + ks * (2 * 108 * 2048), lds0 + slot * P4_W + w4 * 7168);
      dma_run<3, true>(vo, dws, wgs + ks * (2 * 108 * 2048) + 4096, lds0 + slot * P4_W + w4 * 7168 + 4096);
      slot = slot == 2 ? 0 : slot + 1;
      waitv_bar<11>();
    }
    waitv_bar<0>();
#pragma unroll
    for (int m = 0; m < 2; ++m) {
      const float s1 = add32(add16(__uint_as_float(hs[m][0].x) + __uint_as_float(hs[m][1].x))), s2 = add32(add16(__uint_as_float(hs[m][0].y) + __uint_as_float(hs[m][1].y)));
      const float mean = s1 * (1.0f / D_);
      if (g == 0) *(LAS f32x2*)(L + L_MISC + 896 + (w4 * 32 + m * 16 + r) * 8) = f32x2{mean, fmaxf(s2 * (1.0f / D_) - mean * mean, 0.f) + LN_EPS};
    }
    bar_l();
    STAMP(12);
    if (bid == 0) {
      poll_flags(rws, F_RT + w4 * 64 + lane, tag);
      group_sync(L, 11, 4);
      if (tid == 256) st4(rws, 0, WS_FLAG + F_EPOCH * 128, tag);
    }
    const unsigned wpo = w4 * 7168 + (w4 < 2 ? w4 : 2) * 1024, wds = WS_WD + ct * (54 * 5120) + wpo, wdd = lds0 + wpo;
#define P5_ISSUE(st, slot) do { const unsigned so_ = wds + KS5(st) * P5_STAGE, do_ = wdd + (slot) * P5_STAGE; \
      dma_run<4, true>(vo, dws, so_, do_); if (w4 < 2) dma_run<4, true>(vo, dws, so_ + 4096, do_ + 4096); else dma_run<3, true>(vo, dws, so_ + 4096, do_ + 4096); } while (0)
#pragma unroll 1
    for (int st = 0; st < 5; ++st) {
      if (st == P5_PRE) bar_l();
      P5_ISSUE(st, st);
    }
    if (P5_PRE >= 5) bar_l();
    waitv_bar<21>();
    waitv_bar<14>();
#pragma unroll 1
    for (int st = 5; st < 9; ++st) { P5_ISSUE(st, st - 5); waitv_bar<14>(); }
    waitv_bar<7>(); waitv_bar<0>();
  } else {
    f32x4 acc[7][2][2];
#pragma unroll
    for (int n = 0; n < 7; ++n)
#pragma unroll
      for (int u = 0; u < 2; ++u) { acc[n][u][0] = f32x4{0.f, 0.f, 0.f, 0.f}; acc[n][u][1] = acc[n][u][0]; }
    bf16x8 a0[2], a1[2], w0[7], w1[7];
#define P4_RW(wf, W, kk, half) do { _Pragma("unroll") for (int i = 0; i < 7; ++i) wf[i] = lds16(W + (kk) * 14336 + ((half) * 7 + i) * 1024); } while (0)
#define P4_RA(af, A, kk) do { af[0] = lds16(A + (kk) * 1024); af[1] = lds16(A + 2048 + (kk) * 1024); } while (0)
#define P4_MMA(wf, af, half, nrd) do { _Pragma("unroll") for (int i = 0; i < 7; ++i) { const int t = (half) * 7 + i; \
        acc[t >> 1][t & 1][0] = MFMA16(wf[i], af[0], acc[t >> 1][t & 1][0]); acc[t >> 1][t & 1][1] = MFMA16(wf[i], af[1], acc[t >> 1][t & 1][1]); } \
        _Pragma("unroll") for (int i = 0; i < nrd; ++i) { SGB(0x008, 1); SGB(0x100, 1); } SGB(0x008, 14 - nrd); SB(); } while (0)
    bar_l();
    STAMP(11);
    unsigned junk = 0;
    const unsigned dtv = ((idx & 3) * 60 + w4 * 15 + (lane < 15 ? lane : 14)) * 128;
    P4_RA(a0, L + P4_A0 + w4 * 4096 + vo, 0); P4_RW(w0, L + vo, 0, 0);
    SB();
    int slot = 0;
#pragma unroll 1
    for (int st = 0; st < 10; ++st) {
      const lptr W = L + slot * P4_W + vo, A = L + P4_A0 + slot * P4_A + w4 * 4096 + vo;
      slot = slot == 2 ? 0 : slot + 1;
      const lptr Wn = L + slot * P4_W + vo, An = L + P4_A0 + slot * P4_A + w4 * 4096 + vo;
      P4_RW(w1, W, 0, 1);                     P4_MMA(w0, a0, 0, 7);
      P4_RA(a1, A, 1); P4_RW(w0, W, 1, 0);    P4_MMA(w1, a0, 1, 9);
      P4_RW(w1, W, 1, 1);                     P4_MMA(w0, a1, 0, 7);
      if (DTOUCH && st < 9) asm volatile("buffer_load_dword %0, %1, %2, %3 offen sc1" : "+v"(junk) : "v"(dtv), "s"(dws), "s"(WS_WD + ct * (54 * 5120) + KS5(st) * P5_STAGE) : "memory");
      bar_l();
      P4_RA(a0, An, 0); P4_RW(w0, Wn, 0, 0);  P4_MMA(w1, a1, 1, 9);
    }
    STAMP(12);
    constexpr float C1 = -2.0f * 0.7978845608028654f * 1.4426950408889634f, C2 = C1 * 0.044715f;
    u32x2 gq[2][7];
#pragma unroll
    for (int m = 0; m < 2; ++m) {
      const f32x2 tk = *(LAS f32x2*)(L + L_MISC + 896 + (w4 * 32 + m * 16 + r) * 8);
      const float mean = tk.x, var = tk.y;
      const float sc = __builtin_amdgcn_rsqf(var), c1s = C1 * sc, c2s = C2 * sc * sc * sc;
#pragma unroll
      for (int n = 0; n < 7; ++n) {
        const f32x4 cg = *(LAS f32x4*)(L + L_MISC + (n * 16 + 4 * g) * 4), cu = *(LAS f32x4*)(L + L_MISC + (112 + n * 16 + 4 * g) * 4);
        const f32x4 a = acc[n][0][m] - mean * cg, b = acc[n][1][m] - mean * cu;
        const f32x4 z = a * (a * a * c2s + c1s);
        f32x4 ex;
#pragma unroll
        for (int e = 0; e < 4; ++e) ex[e] = __builtin_amdgcn_exp2f(z[e]);
        const f32x4 dn = ex * var + var;
        f32x4 rc;
#pragma unroll
        for (int e = 0; e < 4; ++e) rc[e] = __builtin_amdgcn_rcpf(dn[e]);
        const f32x4 o = (a * b) * rc;
        gq[m][n] = u32x2{cvtpk(o[0], o[1]), cvtpk(o[2], o[3])};
      }
    }
#pragma unroll
    for (int n = 0; n < 7; ++n) {
      const u32x4 v = pair16(gq[0][n], gq[1][n]);
      if (n < ntile) st16(rws, (g & 1) * (54 * 1024) + (g >> 1) * 256 + r * 16, WS_G + T4 * (54 * 1024) + (t0 + n) * 512, v);
    }
    STAMP(13);
    waitv_bar<0>();
    asm volatile("" :: "v"(junk));
    if (wid == 0) raise_flag(rws, F_G + pn * 16 + cgp, tag);

    if (wid == 0) { poll_flags(rws, lane < 16 ? (int)(F_G + (rt >> 1) * 16 + lane) : -1, tag); lds_signal(L, 10); }
    else lds_wait(L, 10);
    STAMP(14);
    constexpr int WIN = GWIN;
    u32x4 gf[WIN][2];
    const int kh = w4 & 1;
    const unsigned gs = WS_G + (4 * rt + (w4 & 2)) * (54 * 1024) + kh * 3072;
#define G_LOAD(i) do { gf[(i) % WIN][0] = ld16(rws, vo + ((i) % 3) * 1024, gs + KS5((i) / 3) * 6144); gf[(i) % WIN][1] = ld16(rws, vo + ((i) % 3) * 1024, gs + 54 * 1024 + KS5((i) / 3) * 6144); } while (0)
#pragma unroll
    for (int i = 0; i < WIN; ++i) G_LOAD(i);
    SB();
    f32x4 ac[2][5];
#pragma unroll
    for (int n = 0; n < 5; ++n) { ac[0][n] = f32x4{0.f, 0.f, 0.f, 0.f}; ac[1][n] = ac[0][n]; }
    const lptr B5 = L + kh * 15360 + vo;
    bf16x8 wf[2][5];
    bar_l();
#pragma unroll
    for (int n = 0; n < 5; ++n) wf[0][n] = lds16(B5 + n * 1024);
#pragma unroll
    for (int i = 0; i < 27; ++i) {
      if (i % 3 == 0 && i > 0 && i < 24) bar_l();
      if (i + 1 < 27) {
#pragma unroll
        for (int n = 0; n < 5; ++n) wf[(i + 1) & 1][n] = lds16(B5 + (((i + 1) / 3) % 5) * P5_STAGE + (((i + 1) % 3) * 5 + n) * 1024);
      }
#pragma unroll
      for (int n = 0; n < 5; ++n) {
        ac[0][n] = MFMA16(wf[i & 1][n], __builtin_bit_cast(bf16x8, gf[i % WIN][0]), ac[0][n]);
        ac[1][n] = MFMA16(wf[i & 1][n], __builtin_bit_cast(bf16x8, gf[i % WIN][1]), ac[1][n]);
      }
      if (i + 1 < 27) { _Pragma("unroll") for (int n = 0; n < 5; ++n) { SGB(0x008, 1); SGB(0x100, 1); SGB(0x008, 1); } }
      SB();
      if (i + WIN < 27) { G_LOAD(i + WIN); SB(); }
    }
    STAMP(15);
#pragma unroll
    for (int n = 0; n < 5; ++n) *(LAS f32x4*)(L + (w4 ^ 1) * 5120 + n * 1024 + vo) = kh ? ac[0][n] : ac[1][n];
    bar_l();
#pragma unroll
    for (int n = 0; n < 5; ++n) ac[0][n] = (kh ? ac[1][n] : ac[0][n]) + *(LAS f32x4*)(L + w4 * 5120 + n * 1024 + vo);
    asm volatile(".Lmega_tail:");
    u32x2 oq[5];
#pragma unroll
    for (int n = 0; n < 5; ++n) oq[n] = u32x2{cvtpk(ac[0][n][0] + bflo(hreg[n].x), ac[0][n][1] + bfhi(hreg[n].x)), cvtpk(ac[0][n][2] + bflo(hreg[n].y), ac[0][n][3] + bfhi(hreg[n].y))};
    u16* op = out + (size_t)(16 * T5 + r) * D_ + 80 * ct + 16 * (g & 1) + 8 * (g >> 1);
    *(u32x4*)op = pair16(oq[0], oq[1]);
    *(u32x4*)(op + 32) = pair16(oq[2], oq[3]);
    const u32x4 last = pair16(oq[4], oq[4]);
    if (!(g & 1)) *(u32x4*)(op + 64) = last;
    STAMP(16);
  }
}

extern "C" void kernel_launch(void* const* d_in, const int* in_sizes, int n_in, void* d_out, int out_size, void* d_ws, size_t ws_size,
                              hipStream_t stream) {
  (void)hipFuncSetAttribute((const void*)mega, hipFuncAttributeMaxDynamicSharedMemorySize, LDS_BYTES);
  mega<<<256, 512, LDS_BYTES, stream>>>((const u16*)d_in[0], (const u16*)d_in[1], (const u16*)d_in[2], (const u16*)d_in[4], (const u16*)d_in[5],
                                        (const u16*)d_in[6], (u16*)d_out, (char*)d_ws);
}
```

```cpp
#include <hip/hip_runtime.h>
#include <hip/hip_bf16.h>
#include <stdint.h>
#define EPFIRST

#define LAS __attribute__((address_space(3)))
#define GAS __attribute__((address_space(1)))
typedef short bf16x8 __attribute__((ext_vector_type(8)));
typedef short s16x4 __attribute__((ext_vector_type(4)));
typedef __bf16 bf16x2 __attribute__((ext_vector_type(2)));
typedef float f32x2 __attribute__((ext_vector_type(2)));
typedef float f32x4 __attribute__((ext_vector_type(4)));
typedef float f32x16 __attribute__((ext_vector_type(16)));
typedef unsigned u32x2 __attribute__((ext_vector_type(2)));
typedef unsigned u32x4 __attribute__((ext_vector_type(4)));
typedef unsigned long long u64;
typedef unsigned short u16;
typedef LAS char* lptr;

constexpr int D_ = 640;
constexpr float LN_EPS = 1e-5f;
constexpr float QSCALE = 0.22360679774997896f * 1.4426950408889634f;

constexpr unsigned WS_FLAG = 0;
constexpr unsigned F_Q = 0, F_O = 256, F_H = 512, F_G = 768, F_RT = 1024, F_EPOCH = 1280;
constexpr unsigned WS_QKV = 262144;
constexpr unsigned WS_O   = WS_QKV + 3 * 32 * 128 * 640;
constexpr unsigned WS_H   = WS_O + 128 * 20 * 1024;
constexpr unsigned WS_G   = WS_H + 128 * 20 * 1024;
constexpr unsigned WS_WO  = WS_G + 128 * 54 * 1024;
constexpr unsigned WS_WD  = WS_WO + 8 * 20 * 5 * 1024;
constexpr unsigned WS_WGU = WS_WD + 8 * 54 * 5 * 1024;
constexpr unsigned WS_HST = WS_WGU + 20 * 108 * 2 * 1024 + 4096;
constexpr unsigned WS_CS  = WS_HST + 128 * 8 * 16 * 8;
constexpr unsigned WS_S1  = WS_CS + 4 * 2 * 1728 * 4 + 4096;
constexpr unsigned WS_C1  = WS_S1 + 2048 * 16;
constexpr unsigned WS_END = WS_C1 + 15 * 10 * 1024;

constexpr int LDS_BYTES = 163840;
constexpr int L_MISC = 161792;
constexpr int L_CNT = LDS_BYTES - 64;
constexpr int P1_STAGE = 32768, L_RT = 3 * P1_STAGE, L_GUCS = 159744;
constexpr int A_K = 0, A_V = 20480, A_Q = 40960, A_ONE = 51200, L_WO = 57344;
constexpr int P4_W = 28672, P4_A0 = 3 * P4_W, P4_A = 16384;
constexpr int P5_STAGE = 30720;

#define STAMP(i) do {} while (0)

#define ROT 9
#define TOUCH 0
#define WARM 1
#define RTOUCH 1
#define DTOUCH 0
#define GWIN 12
#define LPRIO 2
#define P5_PRE 5
#define P1_ROT ((ROT & 1) ? ((idx >> 2) + 2 * (idx & 3)) % 10 : 0)
#define P4_ROT ((ROT & 4) ? ((idx >> 2) + 2 * (idx & 3)) % 10 : 0)
__device__ __forceinline__ unsigned cvtpk(float lo, float hi) { return __builtin_bit_cast(unsigned, __builtin_convertvector(f32x2{lo, hi}, bf16x2)); }
__device__ __forceinline__ float bflo(unsigned v) { return __uint_as_float(v << 16); }
__device__ __forceinline__ float bfhi(unsigned v) { return __uint_as_float(v & 0xffff0000u); }
__device__ __forceinline__ s16x4 trread(lptr p) { return __builtin_amdgcn_ds_read_tr16_b64_v4i16((LAS s16x4*)p); }
__device__ __forceinline__ bf16x8 cat8(s16x4 a, s16x4 b) { return __builtin_shufflevector(a, b, 0, 1, 2, 3, 4, 5, 6, 7); }
__device__ __forceinline__ bf16x8 lds16(lptr p) { return *(LAS bf16x8*)p; }
#define MFMA16(a, b, c) __builtin_amdgcn_mfma_f32_16x16x32_bf16(a, b, c, 0, 0, 0)
#define MFMA32(a, b, c) __builtin_amdgcn_mfma_f32_32x32x16_bf16(a, b, c, 0, 0, 0)
#define MFMA32K8(a, b, c) __builtin_amdgcn_mfma_f32_32x32x8bf16_1k(a, b, c, 0, 0, 0)
#define SB() __builtin_amdgcn_sched_barrier(0)
#define SGB(mask, n) __builtin_amdgcn_sched_group_barrier(mask, n, 0)
__device__ __forceinline__ unsigned rfl(unsigned v) { return __builtin_amdgcn_readfirstlane(v); }
template <int CTRL> __device__ __forceinline__ float dppf(float v) { return __uint_as_float(__builtin_amdgcn_update_dpp(0u, __float_as_uint(v), CTRL, 0xf, 0xf, true)); }
__device__ __forceinline__ float add16(float v) { const auto t = __builtin_amdgcn_permlane16_swap(__float_as_uint(v), __float_as_uint(v), false, false); return __uint_as_float(t[0]) + __uint_as_float(t[1]); }
__device__ __forceinline__ float add32(float v) { const auto t = __builtin_amdgcn_permlane32_swap(__float_as_uint(v), __float_as_uint(v), false, false); return __uint_as_float(t[0]) + __uint_as_float(t[1]); }
__device__ __forceinline__ float sum32(float v) { v += dppf<0xB1>(v); v += dppf<0x4E>(v); v += dppf<0x141>(v); v += dppf<0x140>(v); return add16(v); }
__device__ __forceinline__ float bfr(float v) { return bflo(cvtpk(v, v)); }
__device__ __forceinline__ u32x4 mkdesc(const void* p, unsigned bytes) { const u64 a = (u64)p; return u32x4{(unsigned)a, (unsigned)(a >> 32) & 0xffffu, bytes, 0x00020000u}; }
template <int N> __device__ __forceinline__ void waitv() { asm volatile("s_waitcnt vmcnt(%0)" :: "i"(N) : "memory"); }
template <int N> __device__ __forceinline__ void waitv_bar() { SB(); asm volatile("s_waitcnt vmcnt(%0) lgkmcnt(0)\n\ts_barrier" :: "i"(N) : "memory"); SB(); }
__device__ __forceinline__ void bar_l() { SB(); asm volatile("s_waitcnt lgkmcnt(0)\n\ts_barrier" ::: "memory"); SB(); }

#define DMA1(o, sc) "buffer_load_dwordx4 %0, %1, %2 offen offset:" #o sc " lds\n\t"
template <int N, bool SC1> __device__ __forceinline__ void dma_run(unsigned voff, u32x4 d, unsigned soff, unsigned lds) {
#define DMA_ASM(body) asm volatile("s_mov_b32 m0, %3\n\ts_nop 0\n\t" body :: "v"(voff), "s"(d), "s"(soff), "s"(lds) : "memory")
  if constexpr (SC1) {
    if constexpr (N == 1) DMA_ASM(DMA1(0, " sc1"));
    if constexpr (N == 2) DMA_ASM(DMA1(0, " sc1") DMA1(1024, " sc1"));
    if constexpr (N == 3) DMA_ASM(DMA1(0, " sc1") DMA1(1024, " sc1") DMA1(2048, " sc1"));
    if constexpr (N == 4) DMA_ASM(DMA1(0, " sc1") DMA1(1024, " sc1") DMA1(2048, " sc1") DMA1(3072, " sc1"));
  } else {
    if constexpr (N == 1) DMA_ASM(DMA1(0, ""));
    if constexpr (N == 2) DMA_ASM(DMA1(0, "") DMA1(1024, ""));
    if constexpr (N == 3) DMA_ASM(DMA1(0, "") DMA1(1024, "") DMA1(2048, ""));
    if constexpr (N == 4) DMA_ASM(DMA1(0, "") DMA1(1024, "") DMA1(2048, "") DMA1(3072, ""));
  }
#undef DMA_ASM
}
__device__ __forceinline__ void dma4v(const unsigned (&v)[4], u32x4 d, unsigned soff, unsigned lds) {
  asm volatile("s_mov_b32 m0, %6\n\ts_nop 0\n\t"
               "buffer_load_dwordx4 %0, %4, %5 offen lds\n\t"
               "buffer_load_dwordx4 %1, %4, %5 offen offset:1024 lds\n\t"
               "buffer_load_dwordx4 %2, %4, %5 offen offset:2048 lds\n\t"
               "buffer_load_dwordx4 %3, %4, %5 offen offset:3072 lds"
               :: "v"(v[0]), "v"(v[1]), "v"(v[2]), "v"(v[3]), "s"(d), "s"(soff), "s"(lds) : "memory");
}

typedef __amdgpu_buffer_rsrc_t rsrc_t;
__device__ __forceinline__ void st8(rsrc_t r, unsigned voff, unsigned soff, u32x2 v) { __builtin_amdgcn_raw_buffer_store_b64(v, r, voff, soff, 16); }
__device__ __forceinline__ void st16(rsrc_t r, unsigned voff, unsigned soff, u32x4 v) { __builtin_amdgcn_raw_buffer_store_b128(v, r, voff + soff, 0, 16); }
__device__ __forceinline__ void st4(rsrc_t r, unsigned voff, unsigned soff, unsigned v) { __builtin_amdgcn_raw_buffer_store_b32(v, r, voff, soff, 16); }
__device__ __forceinline__ u32x4 ld16(rsrc_t r, unsigned voff, unsigned soff) { return __builtin_amdgcn_raw_buffer_load_b128(r, voff, soff, 16); }
__device__ __forceinline__ u32x2 ld8(rsrc_t r, unsigned voff, unsigned soff) { return __builtin_amdgcn_raw_buffer_load_b64(r, voff, soff, 16); }
__device__ __forceinline__ unsigned ld4(rsrc_t r, unsigned voff, unsigned soff) { return __builtin_amdgcn_raw_buffer_load_b32(r, voff, soff, 16); }

__device__ __forceinline__ unsigned read_tag(unsigned epoch_v, float dep) { unsigned t; asm volatile("s_nop 0\n\tv_readfirstlane_b32 %0, %1\n\ts_nop 4" : "=s"(t) : "v"(epoch_v), "v"(dep)); return t + 1u; }
__device__ __forceinline__ u32x2 flagval(unsigned tag) { return u32x2{tag, tag * 0x9E3779B1u + 0x7F4A7C15u}; }
template <unsigned STR = 128u>
__device__ __forceinline__ void poll_flags(rsrc_t r, int fidx, unsigned tag) {
  const u32x2 want = flagval(tag);
  const unsigned off = (unsigned)(fidx < 0 ? 0 : fidx) * STR;
#pragma unroll 1
  for (int spin = 0; spin < (1 << 16); ++spin) {
    const u32x2 v = ld8(r, off, WS_FLAG);
    if (__all(fidx < 0 || (v.x == want.x && v.y == want.y))) break;
    __builtin_amdgcn_s_sleep(1);
  }
  __builtin_amdgcn_fence(__ATOMIC_ACQUIRE, "wavefront");
  asm volatile("" ::: "memory");
}
__device__ __forceinline__ bool arrive_last(lptr L, int cnt, unsigned n) {
  asm volatile("s_waitcnt vmcnt(0) lgkmcnt(0)" ::: "memory");
  unsigned old = 0;
  if ((threadIdx.x & 63) == 0) old = __hip_atomic_fetch_add((LAS unsigned*)(L + L_CNT + 4 * cnt), 1u, __ATOMIC_RELAXED, __HIP_MEMORY_SCOPE_WORKGROUP);
  old = rfl(old);
  asm volatile("" ::: "memory");
  return old == n - 1;
}
__device__ __forceinline__ void group_sync(lptr L, int cnt, unsigned n) {
  asm volatile("s_waitcnt lgkmcnt(0)" ::: "memory");
  if ((threadIdx.x & 63) == 0) __hip_atomic_fetch_add((LAS unsigned*)(L + L_CNT + 4 * cnt), 1u, __ATOMIC_RELAXED, __HIP_MEMORY_SCOPE_WORKGROUP);
#pragma unroll 1
  for (int spin = 0; spin < (1 << 22); ++spin) { if (rfl(*(volatile LAS unsigned*)(L + L_CNT + 4 * cnt)) >= n) break; }
  asm volatile("" ::: "memory");
}
__device__ __forceinline__ void raise_flag(rsrc_t r, unsigned fidx, unsigned tag) { if ((threadIdx.x & 63) == 0) st8(r, 0, WS_FLAG + fidx * 128u, flagval(tag)); }
__device__ __forceinline__ void lds_signal(lptr L, int cnt) { asm volatile("" ::: "memory"); if ((threadIdx.x & 63) == 0) *(volatile LAS unsigned*)(L + L_CNT + 4 * cnt) = 1u; }
__device__ __forceinline__ void lds_count(lptr L, int cnt) {
  asm volatile("s_waitcnt lgkmcnt(0)" ::: "memory");
  if ((threadIdx.x & 63) == 0) __hip_atomic_fetch_add((LAS unsigned*)(L + L_CNT + 4 * cnt), 1u, __ATOMIC_RELAXED, __HIP_MEMORY_SCOPE_WORKGROUP);
}
__device__ __forceinline__ void lds_wait(lptr L, int cnt, unsigned n = 1) {
#pragma unroll 1
  for (int spin = 0; spin < (1 << 22); ++spin) { if (rfl(*(volatile LAS unsigned*)(L + L_CNT + 4 * cnt)) >= n) break; __builtin_amdgcn_s_sleep(2); }
  asm volatile("" ::: "memory");
}

__device__ __forceinline__ u32x4 pair16(u32x2 a, u32x2 b) {
  const auto x = __builtin_amdgcn_permlane16_swap(a.x, b.x, false, false), y = __builtin_amdgcn_permlane16_swap(a.y, b.y, false, false);
  return u32x4{x[0], y[0], x[1], y[1]};
}
__device__ __forceinline__ void retile_read(lptr u, int lane, u32x4 (&f)[4]) {
  const lptr rp = u + (8 * (lane >> 4) + ((lane & 15) >> 2)) * 128 + (lane & 3) * 8;
#pragma unroll
  for (int n = 0; n < 4; ++n) f[n] = __builtin_bit_cast(u32x4, cat8(trread(rp + n * 32), trread(rp + n * 32 + 512)));
}

__host__ __device__ constexpr u64 above_mask(int e) {
  u64 m = 0;
  for (int l = 0; l < 64; ++l) if ((e & 3) + 8 * (e >> 2) + 4 * (l >> 5) > (l & 31)) m |= 1ull << l;
  return m;
}

__global__ __launch_bounds__(512) __attribute__((amdgpu_waves_per_eu(2, 2)))
void mega(const u16* __restrict__ x, const u16* __restrict__ wqkv, const u16* __restrict__ wo, const u16* __restrict__ wg, const u16* __restrict__ wu,
          const u16* __restrict__ wd, u16* __restrict__ out, char* __restrict__ ws) {
  extern __shared__ __attribute__((aligned(16))) char smem[];
  const lptr L = (lptr)smem;
  const unsigned lds0 = (unsigned)(uintptr_t)L;
  const int bid = blockIdx.x, xcd = bid & 7, idx = bid >> 3;
  const int tid = threadIdx.x, lane = tid & 63, wid = rfl(tid >> 6), w4 = wid & 3;
  const bool isC = wid < 4;
  const int r = lane & 15, g = lane >> 4;
  const unsigned vo = lane * 16;
  const rsrc_t rws = __builtin_amdgcn_make_buffer_rsrc(ws, 0, WS_END, 0x00020000);
  const u32x4 dws = mkdesc(ws, WS_END);
  STAMP(0);
  const int tm = 4 * (xcd >> 1) + (idx & 3), tn = (8 * (xcd & 1) + (idx >> 2)) ^ (idx >= 30 ? 8 : 0);
  const int wgid = tm * 16 + tn;
  const u32x4 dx = mkdesc(x, (isC || tn < 15) ? 2048 * 1280 : 0), dw = mkdesc(wqkv, tn < 15 ? 640 * 3840 : 0);
  const int srow = 128 * tm + 8 * tn + 2 * w4 + (lane >> 5);
  const int ks0 = (tm + (ROT & 1 ? tn : 0)) % 10;
  unsigned voffX[4], voffW[4];
#pragma unroll
  for (int i = 0; i < 4; ++i) {
    const int p = w4 * 4 + i;
    voffX[i] = (unsigned)((8 * p + (lane >> 3)) * 1280 + (((lane & 7) ^ ((lane >> 4) | (4 * (p & 1)))) * 16) - 1024 * i);
    voffW[i] = (unsigned)((4 * p + (lane >> 4)) * 3840 + ((((((lane & 15) >> 2) ^ (lane >> 4)) << 2) | (lane & 3)) * 16) - 1024 * i);
  }
  const unsigned xs0 = tm * 128 * 1280, ws0 = tn * 256, ld0 = lds0 + w4 * 4096;
  int ks = ks0, slot1 = 0;
#define P1_ISSUE() do { dma4v(voffX, dx, xs0 + ks * 128, ld0 + slot1 * P1_STAGE); dma4v(voffW, dw, ws0 + ks * (64 * 3840), ld0 + slot1 * P1_STAGE + 16384); \
    ks = ks == 9 ? 0 : ks + 1; slot1 = slot1 == 2 ? 0 : slot1 + 1; } while (0)
  unsigned epoch_v;
  asm volatile("buffer_load_dword %0, off, %1, %2 sc1" : "=v"(epoch_v) : "s"(dws), "s"(WS_FLAG + F_EPOCH * 128) : "memory");
  u32x2 xv0 = {0u, 0u}, xv1 = xv0, xv2 = xv0, xv3 = xv0, xv4 = xv0;
  if (isC) {
    asm volatile("buffer_load_dwordx2 %0, %5, %6, 0 offen\n\tbuffer_load_dwordx2 %1, %5, %6, 0 offen offset:256\n\tbuffer_load_dwordx2 %2, %5, %6, 0 offen offset:512\n\t"
                 "buffer_load_dwordx2 %3, %5, %6, 0 offen offset:768\n\tbuffer_load_dwordx2 %4, %5, %6, 0 offen offset:1024"
                 : "+v"(xv0), "+v"(xv1), "+v"(xv2), "+v"(xv3), "+v"(xv4) : "v"(srow * 1280 + (lane & 31) * 8), "s"(dx) : "memory");
  } else { __builtin_amdgcn_s_setprio(LPRIO); P1_ISSUE(); }
  if (tid < 16) *(LAS unsigned*)(L + L_CNT + 4 * tid) = 0u;
  unsigned tag = 0;
  LAS f32x2* st_tok = (LAS f32x2*)(L + L_MISC);
  LAS float* st_col = (LAS float*)(L + L_MISC + 1024);
  if (isC) {
    asm volatile("s_waitcnt vmcnt(0)" : "+v"(xv0), "+v"(xv1), "+v"(xv2), "+v"(xv3), "+v"(xv4) :: "memory");
    const u32x2 xv[5] = {xv0, xv1, xv2, xv3, xv4};
    float s1 = 0.f, s2 = 0.f;
#pragma unroll
    for (int i = 0; i < 5; ++i) s1 += (bflo(xv[i].x) + bfhi(xv[i].x)) + (bflo(xv[i].y) + bfhi(xv[i].y));
    const float mean = sum32(s1) * (1.0f / D_);
#pragma unroll
    for (int i = 0; i < 5; ++i) { const float a = bflo(xv[i].x) - mean, b = bfhi(xv[i].x) - mean, c = bflo(xv[i].y) - mean, d = bfhi(xv[i].y) - mean; s2 += (a * a + b * b) + (c * c + d * d); }
    const float sd = bfr(__builtin_amdgcn_sqrtf(bfr(bfr(sum32(s2) * (1.0f / D_)) + LN_EPS)));
    tag = read_tag(epoch_v, sd);
    const unsigned gt = flagval(tag).y;
    if ((lane & 31) == 0) st16(rws, srow * 16, WS_S1, u32x4{__float_as_uint(bfr(mean)), gt, __float_as_uint(__builtin_amdgcn_rcpf(sd)), gt});
  }
  if (tn < 15) {
    if (!isC) {
      P1_ISSUE();
      waitv_bar<8>();
      tag = read_tag(epoch_v, 0.f);
      const unsigned gt = flagval(tag).y;
      P1_ISSUE();
      if (tm < 10) {
        const bf16x8 ones = {0x3F80, 0x3F80, 0x3F80, 0x3F80, 0x3F80, 0x3F80, 0x3F80, 0x3F80};
        const lptr cp = L + 16384 + (8 * g + (r >> 2)) * 256 + ((w4 ^ (r >> 2)) * 64) + (lane & 3) * 8;
        f32x4 c[2];
#pragma unroll
        for (int hf = 0; hf < 2; ++hf) {
          c[hf] = MFMA16(cat8(trread(cp + hf * 32), trread(cp + hf * 32 + 1024)), ones, (f32x4{0.f, 0.f, 0.f, 0.f}));
          c[hf] = MFMA16(cat8(trread(cp + hf * 32 + 8192), trread(cp + hf * 32 + 9216)), ones, c[hf]);
        }
        const f32x4 cc = (r & 2) ? c[1] : c[0];
        if (r < 4) st16(rws, (r >> 1) * 128 + g * 32 + (r & 1) * 16, WS_C1 + (tn * 10 + tm) * 1024 + w4 * 256,
                        u32x4{__float_as_uint((r & 1) ? cc[2] : cc[0]), gt, __float_as_uint((r & 1) ? cc[3] : cc[1]), gt});
      }
      waitv_bar<8>();
#pragma unroll 1
      for (int st = 3; st < 8; ++st) { P1_ISSUE(); waitv_bar<8>(); }
      u32x4 gs, gc0, gc1, gc2;
      const unsigned gsv = (128 * tm + 32 * w4 + (lane & 31)) * 16 + WS_S1, gcv = WS_C1 + tn * 10240 + g * 1024 + w4 * 256 + r * 16, gcw = gcv + (g < 2 ? 8192 : 0);
      P1_ISSUE();
      asm volatile("buffer_load_dwordx4 %0, %4, %7, 0 offen sc1\n\tbuffer_load_dwordx4 %1, %5, %7, 0 offen sc1\n\tbuffer_load_dwordx4 %2, %5, %7, %8 offen sc1\n\tbuffer_load_dwordx4 %3, %6, %7, 0 offen sc1"
                   : "=&v"(gs), "=&v"(gc0), "=&v"(gc1), "=&v"(gc2) : "v"(gsv), "v"(gcv), "v"(gcw), "s"(dws), "s"(4096u) : "memory");
      waitv_bar<12>();
      P1_ISSUE();
      waitv_bar<12>();
      asm volatile("s_waitcnt vmcnt(0) lgkmcnt(0)\n\ts_barrier" : "+v"(gs), "+v"(gc0), "+v"(gc1), "+v"(gc2) :: "memory");
#pragma unroll 1
      for (int spin = 0; spin < (1 << 16); ++spin) {
        if (__all(gs.y == gt && gs.w == gt && gc0.y == gt && gc0.w == gt && gc1.y == gt && gc1.w == gt && gc2.y == gt && gc2.w == gt)) break;
        gs = ld16(rws, gsv, 0); gc0 = ld16(rws, gcv, 0); gc1 = ld16(rws, gcv, 4096); gc2 = ld16(rws, gcw, 0);
      }
      if (lane < 32) st_tok[32 * w4 + lane] = f32x2{__uint_as_float(gs.x), __uint_as_float(gs.z)};
      { const float ca = add32(add16(__uint_as_float(gc0.x) + __uint_as_float(gc1.x) + (g < 2 ? __uint_as_float(gc2.x) : 0.f)));
        const float cb = add32(add16(__uint_as_float(gc0.z) + __uint_as_float(gc1.z) + (g < 2 ? __uint_as_float(gc2.z) : 0.f)));
        if (lane < 16) *(LAS f32x2*)(st_col + w4 * 32 + lane * 2) = f32x2{ca, cb}; }
      bar_l();
    } else {
      const int wr = w4 >> 1, wc = w4 & 1;
      const int t32 = lane & 31, hh = lane >> 5;
      const int xo = (64 * wr + t32) * 128 + ((hh ^ ((t32 >> 1) & 7)) * 16);
      int wo_[2];
#pragma unroll
      for (int n = 0; n < 2; ++n) wo_[n] = 16384 + (8 * hh + (r >> 2)) * 256 + (((2 * wc + n) ^ (r >> 2)) * 64) + (g & 1) * 32 + (lane & 3) * 8;
      f32x16 acc[2][2];
#pragma unroll
      for (int n = 0; n < 2; ++n)
#pragma unroll
        for (int m = 0; m < 2; ++m)
#pragma unroll
          for (int e = 0; e < 16; ++e) acc[n][m][e] = 0.f;
      bf16x8 xa[2][2], wa[2][2], xb[2][2], wb[2][2];
#define P1_READ(xf, wf, B, kk) do { _Pragma("unroll") for (int k = 0; k < 2; ++k) { \
        xf[k][0] = lds16(B + (xo ^ (64 * kk + 32 * k))); \
        wf[k][0] = cat8(trread(B + wo_[0] + 8192 * kk + 4096 * k), trread(B + wo_[0] + 8192 * kk + 4096 * k + 1024)); \
        xf[k][1] = lds16(B + (xo ^ (64 * kk + 32 * k)) + 4096); \
        wf[k][1] = cat8(trread(B + wo_[1] + 8192 * kk + 4096 * k), trread(B + wo_[1] + 8192 * kk + 4096 * k + 1024)); } } while (0)
#define P1_MMA(xf, wf) do { _Pragma("unroll") for (int k = 0; k < 2; ++k) _Pragma("unroll") for (int n = 0; n < 2; ++n) _Pragma("unroll") for (int m = 0; m < 2; ++m) \
        acc[n][m] = MFMA32(wf[k][n], xf[k][m], acc[n][m]); \
        _Pragma("unroll") for (int i = 0; i < 6; ++i) { SGB(0x008, 1); SGB(0x100, 2); } SGB(0x008, 2); } while (0)
      bar_l();
      STAMP(17);
      P1_READ(xa, wa, L, 0);
      SB();
      int slot = 0;
#pragma unroll 1
      for (int st = 0; st < 10; ++st) {
        const lptr B = L + slot * P1_STAGE;
        slot = slot == 2 ? 0 : slot + 1;
        const lptr Bn = L + slot * P1_STAGE;
        P1_READ(xb, wb, B, 1);
        P1_MMA(xa, wa);
        bar_l();
        P1_READ(xa, wa, Bn, 0);
        P1_MMA(xb, wb);
        SB();
      }
      STAMP(1);
      const int tensor = tn / 5;
      const float osc = tensor == 0 ? QSCALE : 1.0f;
      f32x2 ts[2];
#pragma unroll
      for (int m = 0; m < 2; ++m) { ts[m] = st_tok[64 * wr + 32 * m + t32]; ts[m].y *= osc; }
      const unsigned so = WS_QKV + (tensor * 32 * 64 + 4 * tm + 2 * wr) * 1280, dv0 = (g & 1) * 1280 + (r >> 2) * 320 + (r & 3) * 16;
#pragma unroll
      for (int n = 0; n < 2; ++n)
#pragma unroll
        for (int i = 0; i < 4; ++i) {
          const f32x4 cs = *(LAS f32x4*)(st_col + 64 * wc + 32 * n + 8 * i + 4 * hh);
          const int c4 = (tn - 5 * tensor) * 32 + 16 * wc + 8 * n + 2 * i + hh;
          const int head = (c4 * 205) >> 10, dg = c4 - 5 * head;
          const f32x4 v = (f32x4{acc[n][0][4 * i], acc[n][0][4 * i + 1], acc[n][0][4 * i + 2], acc[n][0][4 * i + 3]} - ts[0].x * cs) * ts[0].y;
          const f32x4 w = (f32x4{acc[n][1][4 * i], acc[n][1][4 * i + 1], acc[n][1][4 * i + 2], acc[n][1][4 * i + 3]} - ts[1].x * cs) * ts[1].y;
          st16(rws, dv0 + head * (64 * 1280) + dg * 64, so, pair16(u32x2{cvtpk(v[0], v[1]), cvtpk(v[2], v[3])}, u32x2{cvtpk(w[0], w[1]), cvtpk(w[2], w[3])}));
        }
      STAMP(2);
      if (arrive_last(L, 0, 4)) raise_flag(rws, F_Q + wgid, tag);
    }
  } else {
    if (!isC && WARM) {
      unsigned acc = 0;
      if (w4 == 0) {
        const char* pc; int len;
        asm volatile("s_getpc_b64 %0\n.Lmega_here:\n\ts_mov_b32 %1, .Lmega_tail-.Lmega_here" : "=s"(pc), "=s"(len));
#pragma unroll
        for (int i = 0; i < 5; ++i) { const int o = (i * 64 + lane) * 128; acc += *(const volatile unsigned*)(pc + (o < len ? o : 0)); }
      } else if (w4 == 1) {
        const int o = (lane < 34 ? lane : 33) * 32768;
        acc += *(const volatile u16*)(wg + o) + *(const volatile u16*)(wu + o) + *(const volatile u16*)(wd + o) + *(const volatile u16*)(out + (lane < 40 ? lane : 39) * 32768);
      } else {
#pragma unroll
        for (int i = 0; i < 4; ++i) acc += ld4(rws, ((w4 - 2) * 256 + i * 64 + lane) * 65536u, 0);
      }
      asm volatile("" :: "v"(acc));
    }
    const u32x4 dwo = mkdesc(wo, 640 * 1280);
    const int u0 = tm * 8 + wid;
    unsigned wod[2][4];
#pragma unroll
    for (int k = 0; k < 2; ++k) {
      const int u = (k == 1 && u0 + 128 < 200) ? u0 + 128 : u0, s0 = u / 10, c0 = u - 10 * s0, j = lane >> 3;
      unsigned vd[4];
#pragma unroll
      for (int i = 0; i < 4; ++i) {
        const int ch = 4 * s0 + i;
        const int row = ch < 64 ? 20 * (ch >> 1) + 4 * (ch & 1) + (j & 3) + 8 * (j >> 2) : 20 * (2 * (ch - 64) + (j >> 2)) + 16 + (j & 3);
        vd[i] = row * 1280 + (lane & 7) * 16 - 1024 * i;
        const int t = 4 * c0 + i, sl = t / 5;
        wod[k][i] = WS_WO + ((sl * 20 + s0) * 5 + (t - 5 * sl)) * 1024;
      }
      dma4v(vd, dwo, c0 * 128, lds0 + L_RT - k * 32768 + wid * 4096);
    }
    waitv_bar<0>();
    tag = read_tag(epoch_v, 0.f);
#pragma unroll
    for (int k = 0; k < 2; ++k) {
      u32x4 f[4];
      retile_read(L + L_RT - k * 32768 + wid * 4096, lane, f);
#pragma unroll
      for (int n = 0; n < 4; ++n) st16(rws, vo, wod[k][n], f[n]);
    }
    if (arrive_last(L, 5, 8)) raise_flag(rws, F_Q + wgid, tag);
  }
  STAMP(3);

  const int head = 4 * xcd + (idx >> 3), qb = idx & 7;
  const int rt = 4 * xcd + (idx & 3), ct = idx >> 2;
  if (isC) {
    if (wid == 0) {
      int f = -1;
      if (lane < 24) {
        const int tmi = lane / 6, wh = lane - 6 * tmi, tmm = 2 * qb - 2 + tmi, ten = wh >> 1;
        const int tnn = 5 * ten + ((20 * head + ((wh & 1) ? 19 : 0)) >> 7);
        if (tmm >= 0 && (ten > 0 || tmi >= 2)) f = F_Q + tmm * 16 + tnn;
      } else if (lane < 40) f = F_Q + (lane - 24) * 16 + 15;
      poll_flags(rws, f, tag);
      lds_signal(L, 8);
    } else lds_wait(L, 8);
    STAMP(4);
    const unsigned hb = WS_QKV + (head * 64 + 8 * qb) * 1280;
    const unsigned kv = hb + (32 * 64 * 1280) + (w4 & 1) * 5120 - ((w4 < 2 && qb > 0) ? 10240 : 0);
    if (w4 < 2) dma_run<3, true>(vo, dws, hb + w4 * 3072, lds0 + A_Q + w4 * 3072);
    else dma_run<2, true>(vo, dws, hb + 6144 + (w4 - 2) * 2048, lds0 + A_Q + 6144 + (w4 - 2) * 2048);
    dma_run<4, true>(vo, dws, kv, lds0 + A_K + w4 * 5120); dma_run<1, true>(vo, dws, kv + 4096, lds0 + A_K + w4 * 5120 + 4096);
    dma_run<4, true>(vo, dws, kv + 32 * 64 * 1280, lds0 + A_V + w4 * 5120); dma_run<1, true>(vo, dws, kv + 32 * 64 * 1280 + 4096, lds0 + A_V + w4 * 5120 + 4096);
  } else if (wid == 4) *(LAS u32x4*)(L + A_ONE + vo) = u32x4{0x00003F80u, 0u, 0x00003F80u, 0u};
  __builtin_amdgcn_s_setprio(0);
  u32x4 ma, mb;
#pragma unroll
  for (int e = 0; e < 4; ++e) {
    const int ka = 2 * (e & 1) + 8 * (e >> 1) + 4 * (lane >> 5), kb = ka + 16, c = lane & 31;
    ma[e] = (ka > c ? 0xffffu : 0u) | (ka + 1 > c ? 0xffff0000u : 0u);
    mb[e] = (kb > c ? 0xffffu : 0u) | (kb + 1 > c ? 0xffff0000u : 0u);
  }
  asm volatile("" : "+v"(ma), "+v"(mb));
  waitv_bar<5>();
  STAMP(5);
  const bool gu_ok = bid < 216, d_ok = bid < 180;
  const int ucb = (gu_ok ? bid : 0) >> 2, isup = ucb >= 27, cbb = ucb - 27 * isup;
  const int sA = 5 * (bid & 3) + w4, sB = 5 * (bid & 3) + 4;
  const int ud = (d_ok ? bid : 0) * 3 + (w4 > 0 ? w4 - 1 : 0), dcb = ud / 54, dsx = ud - 54 * dcb;
  unsigned junk = 0;
  const int tld = (isC || w4 == 0) ? 3456 : 1280;
  const char* tp = ((isC || w4 == 0) ? (const char*)(isup ? wu : wg) + cbb * 128 + (isC ? sA : sB) * (32 * 3456) : (const char*)wd + dsx * (32 * 1280) + dcb * 128) + (lane & 7) * tld;
#define RT_TOUCH(i) asm volatile("global_load_dword %0, %1, off" : "+v"(junk) : "v"(tp + (i) * 8 * tld) : "memory")
  {
    const int c = lane & 31, hh = lane >> 5;
    const int nskip = qb == 0 ? 8 - wid : 0;
    const unsigned wos = WS_WO + ct * 102400 + wid * 12288, wod = lds0 + L_WO + wid * 12288;
#define WO_PIECE(i) dma_run<1, true>(vo, dws, wos + (i) * 1024, wod + (i) * 1024)
    bf16x8 q1; u32x2 q2;
    const int ko = wid * 1280 + ((c >> 2) & 3) * 320 + (c & 3) * 16 + (c >> 4) * 8;
#define LD8(p) (*(volatile LAS s16x4*)(p))
    { const lptr qp = L + A_Q + ko;
      q1 = cat8(LD8(qp + 128 * hh), LD8(qp + 128 * hh + 64));
      q2 = __builtin_bit_cast(u32x2, LD8(qp + 256)); if (hh) q2 = u32x2{0x3F80u, 0u}; }
    const lptr kp = L + A_K + ko;
    f32x16 s[9];
    const f32x16 zero16 = {0.f,0.f,0.f,0.f,0.f,0.f,0.f,0.f,0.f,0.f,0.f,0.f,0.f,0.f,0.f,0.f};
    bf16x8 k1[9]; u32x2 k2[9];
#pragma unroll
    for (int jj = 0; jj < 9; ++jj) {
      const int jb = jj == 0 ? 0 : jj == 1 ? 8 : jj - 1;
      k1[jb] = cat8(LD8(kp + jb * 1280 + 128 * hh), LD8(kp + jb * 1280 + 128 * hh + 64));
      k2[jb] = __builtin_bit_cast(u32x2, LD8(kp + jb * 1280 + 256));
    }
    SB();
    float m0, m1;
#pragma unroll
    for (int jj = 0; jj < 11; ++jj) {
      if (jj < 9) {
        const int jb = jj == 0 ? 0 : jj == 1 ? 8 : jj - 1;
        k2[jb].x = hh ? (jb < nskip ? 0xFF7Fu : 0u) : k2[jb].x;
        s[jb] = MFMA32(k1[jb], q1, zero16);
        s[jb] = MFMA32K8(__builtin_bit_cast(s16x4, k2[jb]), __builtin_bit_cast(s16x4, q2), s[jb]);
      }
      if (jj == 3) {
#pragma unroll
        for (int e = 0; e < 16; ++e) s[0][e] = __builtin_amdgcn_inverse_ballot_w64(above_mask(e)) ? s[0][e] : s[8][e];
        m0 = fmaxf(s[0][0], s[0][1]); m1 = fmaxf(s[0][2], s[0][3]);
#pragma unroll
        for (int e = 4; e < 16; e += 4) { m0 = fmaxf(fmaxf(m0, s[0][e]), s[0][e + 1]); m1 = fmaxf(fmaxf(m1, s[0][e + 2]), s[0][e + 3]); }
      }
      if (jj >= 4) {
        const int jb = jj - 3;
#pragma unroll
        for (int e = 0; e < 16; e += 4) { m0 = fmaxf(fmaxf(m0, s[jb][e]), s[jb][e + 1]); m1 = fmaxf(fmaxf(m1, s[jb][e + 2]), s[jb][e + 3]); }
      }
      if (jj & 1) WO_PIECE(jj >> 1);
      SB();
    }
    float mx = fmaxf(m0, m1);
    { auto rr = __builtin_amdgcn_permlane32_swap(__float_as_uint(mx), __float_as_uint(mx), false, false);
      mx = fmaxf(__uint_as_float(rr[0]), __uint_as_float(rr[1])); }
    STAMP(28);
    if (isC) { waitv<5>(); lds_count(L, 7); }
    u32x2 nm;
    { const unsigned hi = __float_as_uint(-mx) & 0xffff0000u; const float r1 = -mx - __uint_as_float(hi);
      const unsigned mid = __float_as_uint(r1) & 0xffff0000u, lo = __float_as_uint(r1 - __uint_as_float(mid));
      nm = hh ? u32x2{0u, 0u} : u32x2{(hi >> 16) | mid, lo >> 16}; }
    const s16x4 one4 = {0x3F80, 0x3F80, 0x3F80, 0x3F80};
#define S_SHIFT(jb) s[jb] = MFMA32K8(one4, __builtin_bit_cast(s16x4, nm), s[jb])
    S_SHIFT(1);
#pragma unroll 1
    for (int spin = 0; spin < (1 << 22); ++spin) { if (rfl(*(volatile LAS unsigned*)(L + L_CNT + 4 * 7)) >= 4u) break; }
    asm volatile("" ::: "memory");
    SB();
    STAMP(29);
    const int gq = r >> 2, gp = lane & 3, up16 = (lane >> 4) & 1;
    const bool vconst = up16 && gp > 0;
    const lptr vb0 = L + (vconst ? A_ONE + 8 + gq * 16 : A_V + wid * 1280 + hh * 320 + (4 * up16 + gp) * 64 + gq * 16);
    const int vstep = vconst ? 0 : 1280;
    f32x16 o = zero16;
    bf16x8 va[2], vb[2], vc, vd;
#define V_READ(a, b, jb) do { const lptr v0 = vb0 + (jb) * vstep; a = cat8(trread(v0), trread(v0 + 640)); b = cat8(trread(v0 + 8), trread(v0 + 648)); } while (0)
    u32x4 pa[2], pb[2];
#pragma unroll
    for (int it = 0; it < 9; ++it) {
      const int jb = it == 7 ? 0 : it + 1;
      if (it < 8) {
        V_READ(va[it & 1], vb[it & 1], jb);
        if (it == 7) V_READ(vc, vd, 8);
        if (it < 7) S_SHIFT(it == 6 ? 0 : it + 2);
        float p[16];
#pragma unroll
        for (int e = 0; e < 16; ++e) p[e] = __builtin_amdgcn_exp2f(s[jb][e]);
        pa[it & 1] = u32x4{cvtpk(p[0], p[1]), cvtpk(p[2], p[3]), cvtpk(p[4], p[5]), cvtpk(p[6], p[7])};
        pb[it & 1] = u32x4{cvtpk(p[8], p[9]), cvtpk(p[10], p[11]), cvtpk(p[12], p[13]), cvtpk(p[14], p[15])};
      }
      if (it >= 1 && it < 8) {
        o = MFMA32(va[(it - 1) & 1], __builtin_bit_cast(bf16x8, pa[(it - 1) & 1]), o);
        o = MFMA32(vb[(it - 1) & 1], __builtin_bit_cast(bf16x8, pb[(it - 1) & 1]), o);
        SGB(0x100, 4); SGB(0x008, 1); SGB(0x402, 5); SGB(0x008, 1); SGB(0x402, 5); SGB(0x008, 1);
      }
      if (it == 8) {
        o = MFMA32(va[1], __builtin_bit_cast(bf16x8, pa[1] & ma), o);
        o = MFMA32(vb[1], __builtin_bit_cast(bf16x8, pb[1] & mb), o);
        o = MFMA32(vc, __builtin_bit_cast(bf16x8, pa[1] & ~ma), o);
        o = MFMA32(vd, __builtin_bit_cast(bf16x8, pb[1] & ~mb), o);
      }
      if (it < 7) WO_PIECE(5 + it);
      if (it < 4) RT_TOUCH(it);
      if (it == 7) dma_run<1, true>(vo, dws, WS_WO + ct * 102400 + 98304 + w4 * 1024, lds0 + L_WO + 98304 + w4 * 1024);
      SB();
    }
    const float rl = __builtin_amdgcn_rcpf(hh ? o[8] : o[12]);
    const unsigned ob = WS_O + (16 * qb + 2 * wid) * 20480, ov = (c >> 4) * 20480 + (c & 15) * 16;
    st16(rws, ov + hh * 256, ob + head * 512, u32x4{cvtpk(o[0] * rl, o[1] * rl), cvtpk(o[2] * rl, o[3] * rl), cvtpk(o[4] * rl, o[5] * rl), cvtpk(o[6] * rl, o[7] * rl)});
    if (hh == 0) st8(rws, ov, ob + 16384 + (head >> 1) * 256 + (head & 1) * 8, u32x2{cvtpk(o[8] * rl, o[9] * rl), cvtpk(o[10] * rl, o[11] * rl)});
    STAMP(6);
  }
  waitv_bar<0>();
  asm volatile("" :: "v"(junk));
  if (wid == 0) raise_flag(rws, F_O + qb * 32 + head, tag);
  STAMP(7);

  const int pn = 4 * (xcd >> 1) + (idx & 3), cgp = 8 * (xcd & 1) + (idx >> 2);
  const int t0 = cgp < 12 ? 7 * cgp : 6 * cgp + 12, ntile = cgp < 12 ? 7 : 6;
  const int T5 = 4 * rt + w4;
  u32x2 hreg[5];
  const unsigned wgs = WS_WGU + t0 * 2048 + (w4 >> 1) * (108 * 2048) + (w4 & 1) * 7168;
  const int rot4 = P4_ROT;
#define KS4(st) ((st) + rot4 >= 10 ? (st) + rot4 - 10 : (st) + rot4)
  if (isC) {
    u32x2 xv[5];
#pragma unroll
    for (int n = 0; n < 5; ++n) xv[n] = *(const u32x2*)(x + (size_t)(16 * T5 + r) * D_ + 80 * ct + 16 * n + 4 * g);
    if (wid == 0) { poll_flags(rws, lane < 32 ? (int)(F_O + (rt >> 2) * 32 + lane) : -1, tag); lds_signal(L, 9); }
    else lds_wait(L, 9);
    STAMP(8);
    u32x4 af[20];
    const int rot3 = (ROT & 2) ? (ct * 5) >> 3 : 0;
#pragma unroll
    for (int s = 0; s < 20; ++s) { int sg = (s >> 2) + rot3; sg = sg >= 5 ? sg - 5 : sg; af[s] = ld16(rws, vo + (s & 3) * 1024, WS_O + T5 * 20480 + sg * 4096); }
    SB();
    f32x4 acc[5];
#pragma unroll
    for (int n = 0; n < 5; ++n) acc[n] = f32x4{0.f, 0.f, 0.f, 0.f};
    bf16x8 wf[2][5];
    lptr B = L + L_WO + vo + rot3 * 20480;
#pragma unroll
    for (int n = 0; n < 5; ++n) wf[0][n] = lds16(B + n * 1024);
    SB();
#pragma unroll
    for (int s = 0; s < 20; ++s) {
      if (s + 1 < 20) {
        if (((s + 1) & 3) == 0) { int sg = ((s + 1) >> 2) + rot3; sg = sg >= 5 ? sg - 5 : sg; B = L + L_WO + vo + sg * 20480; }
#pragma unroll
        for (int n = 0; n < 5; ++n) wf[(s + 1) & 1][n] = lds16(B + (((s + 1) & 3) * 5 + n) * 1024);
      }
#pragma unroll
      for (int n = 0; n < 5; ++n) acc[n] = MFMA16(wf[s & 1][n], __builtin_bit_cast(bf16x8, af[s]), acc[n]);
      SB();
    }
    lds_count(L, 13);
    STAMP(9);
    float s1 = 0.f, s2 = 0.f;
#pragma unroll
    for (int n = 0; n < 5; ++n)
      hreg[n] = u32x2{cvtpk(acc[n][0] + bflo(xv[n].x), acc[n][1] + bfhi(xv[n].x)), cvtpk(acc[n][2] + bflo(xv[n].y), acc[n][3] + bfhi(xv[n].y))};
    { const unsigned hv = (g & 1) * 512 + (g >> 1) * 256 + r * 16, hsb = WS_H + T5 * 20480 + ct * 2560;
      st16(rws, hv, hsb, pair16(hreg[0], hreg[1]));
      st16(rws, hv, hsb + 1024, pair16(hreg[2], hreg[3]));
      const u32x4 last = pair16(hreg[4], hreg[4]);
      if (!(g & 1)) st16(rws, hv, hsb + 2048, last); }
#pragma unroll
    for (int n = 0; n < 5; ++n) {
      const float a = bflo(hreg[n].x), b = bfhi(hreg[n].x), c = bflo(hreg[n].y), d = bfhi(hreg[n].y);
      s1 += (a + b) + (c + d); s2 += (a * a + b * b) + (c * c + d * d);
    }
    s1 = add32(add16(s1)); s2 = add32(add16(s2));
    if (g == 0) st8(rws, r * 8, WS_HST + (T5 * 8 + ct) * 128, u32x2{__float_as_uint(s1), __float_as_uint(s2)});
    if (arrive_last(L, 3, 4)) raise_flag(rws, F_H + rt * 8 + ct, tag);
  } else {
    __builtin_amdgcn_s_setprio(LPRIO);
    {
      const u32x4 dgu = mkdesc(isup ? wu : wg, 640 * 3456), dB = w4 == 0 ? dgu : mkdesc(wd, 1728 * 1280);
      const unsigned ldB = w4 == 0 ? 3456 : 1280;
      unsigned vA[4], vB[4];
#pragma unroll
      for (int i = 0; i < 4; ++i) { vA[i] = (8 * i + (lane >> 3)) * 3456 + (lane & 7) * 16 - 1024 * i; vB[i] = (8 * i + (lane >> 3)) * ldB + (lane & 7) * 16 - 1024 * i; }
      dma4v(vA, dgu, sA * (32 * 3456) + cbb * 128, lds0 + w4 * 4096);
      dma4v(vB, dB, w4 == 0 ? sB * (32 * 3456) + cbb * 128 : dsx * (32 * 1280) + dcb * 128, lds0 + 16384 + w4 * 4096);
      waitv<0>();
      STAMP(20);
      u32x4 rtf[2][4];
      retile_read(L + w4 * 4096, lane, rtf[0]);
      retile_read(L + 16384 + w4 * 4096, lane, rtf[1]);
      const bf16x8 ones = {0x3F80, 0x3F80, 0x3F80, 0x3F80, 0x3F80, 0x3F80, 0x3F80, 0x3F80};
#pragma unroll
      for (int n = 0; n < 4; ++n) {
        const int t = 4 * dcb + n, sl = t / 5;
        if (gu_ok) st16(rws, vo, WS_WGU + ((sA * 108 + 4 * cbb + n) * 2 + isup) * 1024, rtf[0][n]);
        if (w4 == 0) { if (gu_ok) st16(rws, vo, WS_WGU + ((sB * 108 + 4 * cbb + n) * 2 + isup) * 1024, rtf[1][n]); }
        else if (d_ok) st16(rws, vo, WS_WD + ((sl * 54 + dsx) * 5 + (t - 5 * sl)) * 1024, rtf[1][n]);
        f32x4 c = MFMA16(__builtin_bit_cast(bf16x8, rtf[0][n]), ones, (f32x4{0.f, 0.f, 0.f, 0.f}));
        if (w4 == 0) c = MFMA16(__builtin_bit_cast(bf16x8, rtf[1][n]), ones, c);
        if (r == 0) *(LAS f32x4*)(L + L_GUCS + w4 * 256 + n * 64 + g * 16) = c;
      }
      group_sync(L, 12, 4);
      if (w4 == 1 && gu_ok) { const LAS float* cp = (const LAS float*)(L + L_GUCS) + lane;
        st4(rws, lane * 4, WS_CS + ((bid & 3) * 3456 + ucb * 64) * 4, __float_as_uint((cp[0] + cp[64]) + (cp[128] + cp[192]))); }
      if (arrive_last(L, 1, 4)) raise_flag(rws, F_RT + bid, tag);
    }
    STAMP(21);
    int f = -1;
    if (lane < 24) { const int b = lane >> 2, iu = b >= 3, cb = (t0 >> 2) + b - 3 * iu; if (cb <= ((t0 + ntile - 1) >> 2)) f = F_RT + (cb + 27 * iu) * 4 + (lane & 3); }
    else if (lane < 60) { const int b = (lane - 24) / 18; f = F_RT + (((5 * ct) >> 2) + b) * 18 + (lane - 24 - 18 * b); }
    if (w4 == 0) { poll_flags(rws, f, tag); lds_signal(L, 6); } else lds_wait(L, 6);
    STAMP(22);
#pragma unroll
    for (int st = 0; st < 2; ++st) {
      dma_run<4, true>(vo, dws, wgs + KS4(st) * (2 * 108 * 2048), lds0 + st * P4_W + w4 * 7168);
      dma_run<3, true>(vo, dws, wgs + KS4(st) * (2 * 108 * 2048) + 4096, lds0 + st * P4_W + w4 * 7168 + 4096);
    }
    const int cc = w4 * 64 + lane;
    if (cc < 224) {
      const unsigned o = (cc >= 112 ? 1728 - 112 : 0) + cc + t0 * 16;
      const float t = (__uint_as_float(ld4(rws, o * 4, WS_CS)) + __uint_as_float(ld4(rws, o * 4, WS_CS + 3456 * 4))) +
                      (__uint_as_float(ld4(rws, o * 4, WS_CS + 2 * 3456 * 4)) + __uint_as_float(ld4(rws, o * 4, WS_CS + 3 * 3456 * 4)));
      *(LAS float*)(L + L_MISC + cc * 4) = t;
    }
#pragma unroll
    for (int n = 0; n < 5; ++n) hreg[n] = u32x2{0u, 0u};
  }
  STAMP(10);

  const int T4 = 8 * pn + 2 * w4;
  const int rot5 = (ROT & 8) ? ct : 0;
#define KS5(st) ((st) + rot5 >= 9 ? (st) + rot5 - 9 : (st) + rot5)
  if (!isC) {
    poll_flags(rws, lane < 8 ? (int)(F_H + (2 * pn + (w4 >> 1)) * 8 + lane) : -1, tag);
    lds_wait(L, 13, 4);
    STAMP(11);
    const unsigned as = WS_H + T4 * 20480, ad = lds0 + P4_A0 + w4 * 4096;
#pragma unroll
    for (int st = 0; st < 2; ++st) { dma_run<2, true>(vo, dws, as + KS4(st) * 2048, ad + st * P4_A); dma_run<2, true>(vo, dws, as + 20480 + KS4(st) * 2048, ad + st * P4_A + 2048); }
    u32x2 hs[2][2];
#pragma unroll
    for (int m = 0; m < 2; ++m) { hs[m][0] = ld8(rws, lane * 8, WS_HST + (T4 + m) * 1024); hs[m][1] = ld8(rws, lane * 8, WS_HST + (T4 + m) * 1024 + 512); }
    waitv_bar<8>();
    int slot = 2;
#pragma unroll 1
    for (int st = 2; st < 10; ++st) {
      const int ks = KS4(st);
      dma_run<2, true>(vo, dws, as + ks * 2048, ad + slot * P4_A); dma_run<2, true>(vo, dws, as + 20480 + ks * 2048, ad + slot * P4_A + 2048);
      dma_run<4, true>(vo, dws, wgs + ks * (2 * 108 * 2048), lds0 + slot * P4_W + w4 * 7168);
      dma_run<3, true>(vo, dws, wgs + ks * (2 * 108 * 2048) + 4096, lds0 + slot * P4_W + w4 * 7168 + 4096);
      slot = slot == 2 ? 0 : slot + 1;
      waitv_bar<11>();
    }
    waitv_bar<0>();
#pragma unroll
    for (int m = 0; m < 2; ++m) {
      const float s1 = add32(add16(__uint_as_float(hs[m][0].x) + __uint_as_float(hs[m][1].x))), s2 = add32(add16(__uint_as_float(hs[m][0].y) + __uint_as_float(hs[m][1].y)));
      const float mean = s1 * (1.0f / D_);
      if (g == 0) *(LAS f32x2*)(L + L_MISC + 896 + (w4 * 32 + m * 16 + r) * 8) = f32x2{mean, fmaxf(s2 * (1.0f / D_) - mean * mean, 0.f) + LN_EPS};
    }
    bar_l();
    STAMP(12);
    if (bid == 0) {
      poll_flags(rws, F_RT + w4 * 64 + lane, tag);
      group_sync(L, 11, 4);
      if (tid == 256) st4(rws, 0, WS_FLAG + F_EPOCH * 128, tag);
    }
    const unsigned wpo = w4 * 7168 + (w4 < 2 ? w4 : 2) * 1024, wds = WS_WD + ct * (54 * 5120) + wpo, wdd = lds0 + wpo;
#define P5_ISSUE(st, slot) do { const unsigned so_ = wds + KS5(st) * P5_STAGE, do_ = wdd + (slot) * P5_STAGE; \
      dma_run<4, true>(vo, dws, so_, do_); if (w4 < 2) dma_run<4, true>(vo, dws, so_ + 4096, do_ + 4096); else dma_run<3, true>(vo, dws, so_ + 4096, do_ + 4096); } while (0)
#pragma unroll 1
    for (int st = 0; st < 5; ++st) {
      if (st == P5_PRE) bar_l();
      P5_ISSUE(st, st);
    }
    if (P5_PRE >= 5) bar_l();
    waitv_bar<21>();
    waitv_bar<14>();
#pragma unroll 1
    for (int st = 5; st < 9; ++st) { P5_ISSUE(st, st - 5); waitv_bar<14>(); }
    waitv_bar<7>(); waitv_bar<0>();
  } else {
    f32x4 acc[7][2][2];
#pragma unroll
    for (int n = 0; n < 7; ++n)
#pragma unroll
      for (int u = 0; u < 2; ++u) { acc[n][u][0] = f32x4{0.f, 0.f, 0.f, 0.f}; acc[n][u][1] = acc[n][u][0]; }
    bf16x8 a0[2], a1[2], w0[7], w1[7];
#define P4_RW(wf, W, kk, half) do { _Pragma("unroll") for (int i = 0; i < 7; ++i) wf[i] = lds16(W + (kk) * 14336 + ((half) * 7 + i) * 1024); } while (0)
#define P4_RA(af, A, kk) do { af[0] = lds16(A + (kk) * 1024); af[1] = lds16(A + 2048 + (kk) * 1024); } while (0)
#define P4_MMA(wf, af, half, nrd) do { _Pragma("unroll") for (int i = 0; i < 7; ++i) { const int t = (half) * 7 + i; \
        acc[t >> 1][t & 1][0] = MFMA16(wf[i], af[0], acc[t >> 1][t & 1][0]); acc[t >> 1][t & 1][1] = MFMA16(wf[i], af[1], acc[t >> 1][t & 1][1]); } \
        _Pragma("unroll") for (int i = 0; i < nrd; ++i) { SGB(0x008, 1); SGB(0x100, 1); } SGB(0x008, 14 - nrd); SB(); } while (0)
    bar_l();
    STAMP(11);
    junk = 0;
    const unsigned dtv = ((idx & 3) * 60 + w4 * 15 + (lane < 15 ? lane : 14)) * 128;
    P4_RA(a0, L + P4_A0 + w4 * 4096 + vo, 0); P4_RW(w0, L + vo, 0, 0);
    SB();
    int slot = 0;
#pragma unroll 1
    for (int st = 0; st < 10; ++st) {
      const lptr W = L + slot * P4_W + vo, A = L + P4_A0 + slot * P4_A + w4 * 4096 + vo;
      slot = slot == 2 ? 0 : slot + 1;
      const lptr Wn = L + slot * P4_W + vo, An = L + P4_A0 + slot * P4_A + w4 * 4096 + vo;
      P4_RW(w1, W, 0, 1);                     P4_MMA(w0, a0, 0, 7);
      P4_RA(a1, A, 1); P4_RW(w0, W, 1, 0);    P4_MMA(w1, a0, 1, 9);
      P4_RW(w1, W, 1, 1);                     P4_MMA(w0, a1, 0, 7);
      if (DTOUCH && st < 9) asm volatile("buffer_load_dword %0, %1, %2, %3 offen sc1" : "+v"(junk) : "v"(dtv), "s"(dws), "s"(WS_WD + ct * (54 * 5120) + KS5(st) * P5_STAGE) : "memory");
      bar_l();
      P4_RA(a0, An, 0); P4_RW(w0, Wn, 0, 0);  P4_MMA(w1, a1, 1, 9);
    }
    STAMP(12);
    constexpr float C1 = -2.0f * 0.7978845608028654f * 1.4426950408889634f, C2 = C1 * 0.044715f;
    u32x2 gq[2][7];
#pragma unroll
    for (int m = 0; m < 2; ++m) {
      const f32x2 tk = *(LAS f32x2*)(L + L_MISC + 896 + (w4 * 32 + m * 16 + r) * 8);
      const float mean = tk.x, var = tk.y;
      const float sc = __builtin_amdgcn_rsqf(var), c1s = C1 * sc, c2s = C2 * sc * sc * sc;
#pragma unroll
      for (int n = 0; n < 7; ++n) {
        const f32x4 cg = *(LAS f32x4*)(L + L_MISC + (n * 16 + 4 * g) * 4), cu = *(LAS f32x4*)(L + L_MISC + (112 + n * 16 + 4 * g) * 4);
        const f32x4 a = acc[n][0][m] - mean * cg, b = acc[n][1][m] - mean * cu;
        const f32x4 z = a * (a * a * c2s + c1s);
        f32x4 ex;
#pragma unroll
        for (int e = 0; e < 4; ++e) ex[e] = __builtin_amdgcn_exp2f(z[e]);
        const f32x4 dn = ex * var + var;
        f32x4 rc;
#pragma unroll
        for (int e = 0; e < 4; ++e) rc[e] = __builtin_amdgcn_rcpf(dn[e]);
        const f32x4 o = (a * b) * rc;
        gq[m][n] = u32x2{cvtpk(o[0], o[1]), cvtpk(o[2], o[3])};
      }
    }
#pragma unroll
    for (int n = 0; n < 7; ++n) {
      const u32x4 v = pair16(gq[0][n], gq[1][n]);
      if (n < ntile) st16(rws, (g & 1) * (54 * 1024) + (g >> 1) * 256 + r * 16, WS_G + T4 * (54 * 1024) + (t0 + n) * 512, v);
    }
    STAMP(13);
    waitv_bar<0>();
    asm volatile("" :: "v"(junk));
    if (wid == 0) raise_flag(rws, F_G + pn * 16 + cgp, tag);

    if (wid == 0) { poll_flags(rws, lane < 16 ? (int)(F_G + (rt >> 1) * 16 + lane) : -1, tag); lds_signal(L, 10); }
    else lds_wait(L, 10);
    STAMP(14);
    constexpr int WIN = GWIN;
    u32x4 gf[WIN][2];
    const int kh = w4 & 1;
    const unsigned gs = WS_G + (4 * rt + (w4 & 2)) * (54 * 1024) + kh * 3072;
#define G_LOAD(i) do { gf[(i) % WIN][0] = ld16(rws, vo + ((i) % 3) * 1024, gs + KS5((i) / 3) * 6144); gf[(i) % WIN][1] = ld16(rws, vo + ((i) % 3) * 1024, gs + 54 * 1024 + KS5((i) / 3) * 6144); } while (0)
#pragma unroll
    for (int i = 0; i < WIN; ++i) G_LOAD(i);
    SB();
    f32x4 ac[2][5];
#pragma unroll
    for (int n = 0; n < 5; ++n) { ac[0][n] = f32x4{0.f, 0.f, 0.f, 0.f}; ac[1][n] = ac[0][n]; }
    const lptr B5 = L + kh * 15360 + vo;
    bf16x8 wf[2][5];
    bar_l();
#pragma unroll
    for (int n = 0; n < 5; ++n) wf[0][n] = lds16(B5 + n * 1024);
#pragma unroll
    for (int i = 0; i < 27; ++i) {
      if (i % 3 == 0 && i > 0 && i < 24) bar_l();
      if (i + 1 < 27) {
#pragma unroll
        for (int n = 0; n < 5; ++n) wf[(i + 1) & 1][n] = lds16(B5 + (((i + 1) / 3) % 5) * P5_STAGE + (((i + 1) % 3) * 5 + n) * 1024);
      }
#pragma unroll
      for (int n = 0; n < 5; ++n) {
        ac[0][n] = MFMA16(wf[i & 1][n], __builtin_bit_cast(bf16x8, gf[i % WIN][0]), ac[0][n]);
        ac[1][n] = MFMA16(wf[i & 1][n], __builtin_bit_cast(bf16x8, gf[i % WIN][1]), ac[1][n]);
      }
      if (i + 1 < 27) { _Pragma("unroll") for (int n = 0; n < 5; ++n) { SGB(0x008, 1); SGB(0x100, 1); SGB(0x008, 1); } }
      SB();
      if (i + WIN < 27) { G_LOAD(i + WIN); SB(); }
    }
    STAMP(15);
#pragma unroll
    for (int n = 0; n < 5; ++n) *(LAS f32x4*)(L + (w4 ^ 1) * 5120 + n * 1024 + vo) = kh ? ac[0][n] : ac[1][n];
    bar_l();
#pragma unroll
    for (int n = 0; n < 5; ++n) ac[0][n] = (kh ? ac[1][n] : ac[0][n]) + *(LAS f32x4*)(L + w4 * 5120 + n * 1024 + vo);
    asm volatile(".Lmega_tail:");
    u32x2 oq[5];
#pragma unroll
    for (int n = 0; n < 5; ++n) oq[n] = u32x2{cvtpk(ac[0][n][0] + bflo(hreg[n].x), ac[0][n][1] + bfhi(hreg[n].x)), cvtpk(ac[0][n][2] + bflo(hreg[n].y), ac[0][n][3] + bfhi(hreg[n].y))};
    const rsrc_t ro = __builtin_amdgcn_make_buffer_rsrc(out, 0, 2048 * 1280, 0x00020000);
    const unsigned ov = ((16 * T5 + r) * D_ + 80 * ct + 16 * (g & 1) + 8 * (g >> 1)) * 2;
#define OAUX 16
    __builtin_amdgcn_raw_buffer_store_b128(pair16(oq[0], oq[1]), ro, ov, 0, OAUX);
    __builtin_amdgcn_raw_buffer_store_b128(pair16(oq[2], oq[3]), ro, ov + 64, 0, OAUX);
    const u32x4 last = pair16(oq[4], oq[4]);
    if (!(g & 1)) __builtin_amdgcn_raw_buffer_store_b128(last, ro, ov + 128, 0, OAUX);
    STAMP(16);
  }
}

#define WSOFF 0
extern "C" void kernel_launch(void* const* d_in, const int* in_sizes, int n_in, void* d_out, int out_size, void* d_ws, size_t ws_size,
                              hipStream_t stream) {
  (void)hipFuncSetAttribute((const void*)mega, hipFuncAttributeMaxDynamicSharedMemorySize, LDS_BYTES);
  mega<<<256, 512, LDS_BYTES, stream>>>((const u16*)d_in[0], (const u16*)d_in[1], (const u16*)d_in[2], (const u16*)d_in[4], (const u16*)d_in[5],
                                        (const u16*)d_in[6], (u16*)d_out, (char*)d_ws + WSOFF);
}
```
